```python
import torch

def _softmax(x, axis=-1):
    return torch.softmax(x, dim=axis)
def _gelu(x):
    return torch.nn.functional.gelu(x, approximate="tanh")
def _silu(x):
    return torch.nn.functional.silu(x)
def _sigmoid(x):
    return torch.sigmoid(x)
def _layernorm(x, axis=-1, eps=1e-5):
    m = x.mean(dim=axis, keepdim=True)
    v = x.var(dim=axis, keepdim=True, unbiased=False)
    return (x - m) / torch.sqrt(v + eps)
def _scatter_add(tgt, idx, src, axis=0):
    return tgt.clone().index_add_(axis, idx, src)
def _segment_sum(src, seg, n):
    out = torch.zeros((n,) + src.shape[1:], dtype=src.dtype)
    return out.index_add_(0, seg, src)
def _pad(x, pw):
    flat = tuple(v for pair in reversed(pw) for v in pair)
    return torch.nn.functional.pad(x, flat)

kernel_name = 'concat_heads__mha_block_cached__gqa_block_cached__near_ridge'

def setup_inputs():

    _w = (lambda a: (lambda t: t.to(torch.bfloat16) if t.is_floating_point() else t)(torch.as_tensor(a)))
    def _A_setup():
        B, Sq, D = 8, 160, 576
        H, Sk, Dh = 32, 512, 128
        return {
            "x": _w((lambda *s: torch.randn(*s, dtype=torch.bfloat16) * 0.8)(B, Sq, D)),
            "k_cache": _w((lambda *s: torch.randn(*s, dtype=torch.bfloat16) * 0.8)(B, H, Sk, Dh)),
            "v_cache": _w((lambda *s: torch.randn(*s, dtype=torch.bfloat16) * 0.05)(B, H, Sk, Dh)),
            "wq": _w((lambda *s: torch.randn(*s, dtype=torch.bfloat16) * 0.8)(D, H * Dh) * (D ** -0.5)),
            "wo": _w((lambda *s: torch.randn(*s, dtype=torch.bfloat16) * 0.05)(H * Dh, D) * ((H * Dh) ** -0.5)),
        }
    _A_in = _A_setup()

    def _B_setup():
        B, Sq, D = 8, 160, 576
        Hq, Hkv, Sk, Dh = 32, 32, 512, 128
        assert Hq % Hkv == 0, (Hq, Hkv)
        return {
            "x": _w((lambda *s: torch.randn(*s, dtype=torch.bfloat16) * 0.8)(B, Sq, D)),
            "k_cache": _w((lambda *s: torch.randn(*s, dtype=torch.bfloat16) * 0.8)(B, Hkv, Sk, Dh)),
            "v_cache": _w((lambda *s: torch.randn(*s, dtype=torch.bfloat16) * 0.05)(B, Hkv, Sk, Dh)),
            "wq": _w((lambda *s: torch.randn(*s, dtype=torch.bfloat16) * 0.8)(D, Hq * Dh) * (D ** -0.5)),
            "wo": _w((lambda *s: torch.randn(*s, dtype=torch.bfloat16) * 0.05)(Hq * Dh, D) * ((Hq * Dh) ** -0.5)),
        }
    _B_in = _B_setup()

    return {
        "A_x": _A_in["x"],
        "A_k_cache": _A_in["k_cache"],
        "A_v_cache": _A_in["v_cache"],
        "A_wq": _A_in["wq"],
        "A_wo": _A_in["wo"],
        "B_x": _B_in["x"],
        "B_k_cache": _B_in["k_cache"],
        "B_v_cache": _B_in["v_cache"],
        "B_wq": _B_in["wq"],
        "B_wo": _B_in["wo"],
    }

def reference(A_x, A_k_cache, A_v_cache, A_wq, A_wo, B_x, B_k_cache, B_v_cache, B_wq, B_wo):
    def _A_ref(x, k_cache, v_cache, wq, wo):
        B, Sq, D = x.shape
        H, Dh = 32, 128
        q = (lambda x, *d: x.permute(*d))((x @ wq).reshape(B, Sq, H, Dh), 0, 2, 1, 3)
        scores = torch.einsum("bhqd,bhkd->bhqk", q, k_cache) * (Dh ** -0.5)
        p = _softmax(scores, axis=-1)
        o = torch.einsum("bhqk,bhkd->bhqd", p, v_cache)
        return (lambda x, *d: x.permute(*d))(o, 0, 2, 1, 3).reshape(B, Sq, H * Dh) @ wo

    def _B_ref(x, k_cache, v_cache, wq, wo):
        B, Sq, D = x.shape
        Hq, Dh = 32, 128
        Hkv = k_cache.shape[1]
        rep = Hq // Hkv
        q = (lambda x, *d: x.permute(*d))((x @ wq).reshape(B, Sq, Hq, Dh), 0, 2, 1, 3)
        k = (lambda x, n, axis: torch.repeat_interleave(x, n, dim=axis))(k_cache, rep, axis=1)
        v = (lambda x, n, axis: torch.repeat_interleave(x, n, dim=axis))(v_cache, rep, axis=1)
        scores = torch.einsum("bhqd,bhkd->bhqk", q, k) * (Dh ** -0.5)
        p = _softmax(scores, axis=-1)
        o = torch.einsum("bhqk,bhkd->bhqd", p, v)
        return (lambda x, *d: x.permute(*d))(o, 0, 2, 1, 3).reshape(B, Sq, Hq * Dh) @ wo

    a = _A_ref(A_x, A_k_cache, A_v_cache, A_wq, A_wo)
    b = _B_ref(B_x, B_k_cache, B_v_cache, B_wq, B_wo)
    return torch.concatenate([a, b], axis=-1)
```

```cpp
#include <hip/hip_runtime.h>
#include <hip/hip_bf16.h>
#include <type_traits>
#include <utility>

typedef __bf16 bf16x8 __attribute__((ext_vector_type(8)));
typedef __bf16 bf16x4 __attribute__((ext_vector_type(4)));
typedef __bf16 bf16x2 __attribute__((ext_vector_type(2)));
typedef float f32x4 __attribute__((ext_vector_type(4)));
typedef float f32x2 __attribute__((ext_vector_type(2)));
typedef unsigned u32x4 __attribute__((ext_vector_type(4)));
typedef unsigned u32x2 __attribute__((ext_vector_type(2)));
#define AS3 __attribute__((address_space(3)))
#define DI __device__ __forceinline__
#define INL __attribute__((always_inline))
#define GAP() __builtin_amdgcn_sched_barrier(0)
typedef __amdgpu_buffer_rsrc_t srd_t;
template <class F, int... Is> DI void sfor_(F&& f, std::integer_sequence<int, Is...>) { (f(std::integral_constant<int, Is>{}), ...); }
template <int N, class F> DI void sfor(F&& f) { sfor_(f, std::make_integer_sequence<int, N>{}); }
template <int V> using IC = std::integral_constant<int, V>;

constexpr int SQ = 160, DM = 576, NH = 32, SK = 512, DH = 128, NB = 8;
constexpr unsigned KV_BYTES = (unsigned)NB * NH * SK * DH * 2;
constexpr unsigned W_BYTES = DM * 4096 * 2;
constexpr unsigned X_BYTES = NB * SQ * DM * 2;
constexpr unsigned HEAD_BYTES = SK * DH * 2;
constexpr unsigned OOB = 0x40000000u;
constexpr float QSCALE = 0.08838834764831845f * 1.4426950408889634f;
constexpr float THR = 10.0f;
#define WQ_NT false
#define X_NT false
#define ROT ((b + 2 * hp + hp / 2) % 9)

constexpr size_t WS_OF = 0;
constexpr size_t OF_BYTES = (size_t)2 * 8 * 32 * 4 * 10 * 1024;
constexpr size_t WS_PART = WS_OF + OF_BYTES;

constexpr unsigned UNIT = 16384;
constexpr unsigned WQB = 3 * UNIT, WQ_BUF = 16448, WQ_HEAD = 8224;
constexpr unsigned XB = WQB + 3 * WQ_BUF, X_BUF = 20480;
static_assert(XB + 3 * X_BUF <= 163840, "lds");

DI srd_t mksrd(const void* p, unsigned bytes) { return __builtin_amdgcn_make_buffer_rsrc((void*)p, (short)0, (int)bytes, 0x00020000); }
template <unsigned IMM> DI void setm0(unsigned base) { asm volatile("s_add_u32 m0, %0, %c1" :: "s"(base), "i"(IMM) : "scc"); }
template <unsigned OFF, bool NT> DI void dmal(unsigned voff, srd_t srd, unsigned soff) {
  if constexpr (NT) asm volatile("buffer_load_dwordx4 %0, %1, %2 offen offset:%c3 nt lds" :: "v"(voff), "s"(srd), "s"(soff), "i"(OFF) : "memory");
  else              asm volatile("buffer_load_dwordx4 %0, %1, %2 offen offset:%c3 lds" :: "v"(voff), "s"(srd), "s"(soff), "i"(OFF) : "memory");
}
template <int A, unsigned OFF> DI void loada(unsigned voff, srd_t srd, unsigned soff) {
  asm volatile("buffer_load_dwordx4 a[%c3:%c4], %0, %1, %2 offen offset:%c5 nt" :: "v"(voff), "s"(srd), "s"(soff), "i"(A), "i"(A + 3), "i"(OFF) : "memory"); }
template <int A, unsigned OFF> DI void dswa(unsigned addr) { asm volatile("ds_write_b128 %0, a[%c1:%c2] offset:%c3" :: "v"(addr), "i"(A), "i"(A + 3), "i"(OFF) : "memory"); }
DI void pin(unsigned& s) { asm volatile("" : "+s"(s)); }
DI void pinv(unsigned& v) { asm volatile("" : "+v"(v)); }
DI void snop0() { asm volatile("s_nop 0"); }
template <int N> DI void wait_vm() { asm volatile("s_waitcnt vmcnt(%0)" :: "i"(N) : "memory"); }
DI void wait_lgkm0() { __builtin_amdgcn_s_waitcnt(0xC07F); }
template <int N> DI void wait_bar() { wait_lgkm0(); asm volatile("s_waitcnt vmcnt(%0)\n\ts_barrier" :: "i"(N) : "memory"); GAP(); }
DI void bar_only() { wait_lgkm0(); asm volatile("s_barrier" ::: "memory"); GAP(); }
DI void nop16() { asm volatile("s_nop 7\n\ts_nop 7" ::: "memory"); GAP(); }
DI bf16x8 lds128(unsigned a) { return *(const AS3 bf16x8*)(size_t)a; }
DI bf16x4 ldstr1(unsigned a) { return __builtin_amdgcn_ds_read_tr16_b64_v4bf16((AS3 bf16x4*)(size_t)a); }
DI bf16x8 cat(bf16x4 lo, bf16x4 hi) { return __builtin_shufflevector(lo, hi, 0, 1, 2, 3, 4, 5, 6, 7); }

#define ACLOB "a0","a1","a2","a3","a4","a5","a6","a7","a8","a9","a10","a11","a12","a13","a14","a15","a16","a17","a18","a19","a20","a21","a22","a23","a24","a25","a26","a27","a28","a29","a30","a31","a32","a33","a34","a35","a36","a37","a38","a39","a40","a41","a42","a43","a44","a45","a46","a47","a48","a49","a50","a51","a52","a53","a54","a55","a56","a57","a58","a59","a60","a61","a62","a63","a64","a65","a66","a67","a68","a69","a70","a71","a72","a73","a74","a75","a76","a77","a78","a79","a80","a81","a82","a83","a84","a85","a86","a87","a88","a89","a90","a91","a92","a93","a94","a95","a96","a97","a98","a99","a100","a101","a102","a103","a104","a105","a106","a107","a108","a109","a110","a111","a112","a113","a114","a115","a116","a117","a118","a119","a120","a121","a122","a123","a124","a125","a126","a127","a128","a129","a130","a131","a132","a133","a134","a135","a136","a137","a138","a139","a140","a141","a142","a143","a144","a145","a146","a147","a148","a149","a150","a151","a152","a153","a154","a155","a156","a157","a158","a159","a160","a161","a162","a163","a164","a165","a166","a167","a168","a169","a170","a171","a172","a173","a174","a175","a176","a177","a178","a179","a180","a181","a182","a183","a184","a185","a186","a187","a188","a189","a190","a191","a192","a193","a194","a195","a196","a197","a198","a199","a200","a201","a202","a203","a204","a205","a206","a207","a208","a209","a210","a211","a212","a213","a214","a215","a216","a217","a218","a219","a220","a221","a222","a223","a224","a225","a226","a227","a228","a229","a230","a231","a232","a233","a234","a235","a236","a237","a238","a239","a240","a241","a242","a243","a244","a245","a246","a247","a248","a249","a250","a251","a252","a253","a254","a255"
constexpr int AO(int t, int qt) { return (t * 5 + qt) * 4; }
constexpr int AQ(int qt, int s) { return 160 + (qt * 4 + s) * 4; }
constexpr int AONES = 240, AL0 = 244;
template <int A> DI void mfa(bf16x8 a, bf16x8 b) { asm volatile("v_mfma_f32_16x16x32_bf16 a[%c2:%c3], %0, %1, a[%c2:%c3]" :: "v"(a), "v"(b), "i"(A), "i"(A + 3)); }
template <int A> DI void mfa0(bf16x8 a, bf16x8 b) { asm volatile("v_mfma_f32_16x16x32_bf16 a[%c2:%c3], %0, %1, 0" :: "v"(a), "v"(b), "i"(A), "i"(A + 3)); }
template <int A> DI void mfl(bf16x8 b) { asm volatile("v_mfma_f32_16x16x32_bf16 a[%c1:%c2], a[%c3:%c4], %0, a[%c1:%c2]" :: "v"(b), "i"(A), "i"(A + 3), "i"(AONES), "i"(AONES + 3)); }
DI void mflv(f32x4& acc, bf16x8 b) { asm volatile("v_mfma_f32_16x16x32_bf16 %0, a[%c2:%c3], %1, %0" : "+v"(acc) : "v"(b), "i"(AONES), "i"(AONES + 3)); }
template <int A> DI void mfs0(f32x4& s, bf16x8 a, f32x4 c) { asm volatile("v_mfma_f32_16x16x32_bf16 %0, %1, a[%c3:%c4], %2" : "=&v"(s) : "v"(a), "v"(c), "i"(A), "i"(A + 3)); }
template <int A> DI void mfsz(f32x4& s, bf16x8 a) { asm volatile("v_mfma_f32_16x16x32_bf16 %0, %1, a[%c2:%c3], 0" : "=&v"(s) : "v"(a), "i"(A), "i"(A + 3)); }
template <int A> DI void mfs(f32x4& s, bf16x8 a) { asm volatile("v_mfma_f32_16x16x32_bf16 %0, %1, a[%c2:%c3], %0" : "+v"(s) : "v"(a), "i"(A), "i"(A + 3)); }
template <int A> DI f32x4 aread(void) { f32x4 r;
  asm volatile("v_accvgpr_read_b32 %0, a%c4\n\tv_accvgpr_read_b32 %1, a%c5\n\tv_accvgpr_read_b32 %2, a%c6\n\tv_accvgpr_read_b32 %3, a%c7"
               : "=v"(r[0]), "=v"(r[1]), "=v"(r[2]), "=v"(r[3]) : "i"(A), "i"(A + 1), "i"(A + 2), "i"(A + 3)); return r; }
template <int A> DI void awrite(u32x4 w) {
  asm volatile("v_accvgpr_write_b32 a%c4, %0\n\tv_accvgpr_write_b32 a%c5, %1\n\tv_accvgpr_write_b32 a%c6, %2\n\tv_accvgpr_write_b32 a%c7, %3"
               :: "v"(w[0]), "v"(w[1]), "v"(w[2]), "v"(w[3]), "i"(A), "i"(A + 1), "i"(A + 2), "i"(A + 3)); }
template <int A> DI void azero() { asm volatile("v_accvgpr_write_b32 a%c0, 0\n\tv_accvgpr_write_b32 a%c1, 0\n\tv_accvgpr_write_b32 a%c2, 0\n\tv_accvgpr_write_b32 a%c3, 0" :: "i"(A), "i"(A + 1), "i"(A + 2), "i"(A + 3)); }
template <int A> DI void ascale(float f) { float t0, t1;
  asm volatile("v_accvgpr_read_b32 %0, a%c3\n\tv_accvgpr_read_b32 %1, a%c4\n\tv_mul_f32 %0, %2, %0\n\tv_mul_f32 %1, %2, %1\n\tv_accvgpr_write_b32 a%c3, %0\n\tv_accvgpr_write_b32 a%c4, %1\n\t"
               "v_accvgpr_read_b32 %0, a%c5\n\tv_accvgpr_read_b32 %1, a%c6\n\tv_mul_f32 %0, %2, %0\n\tv_mul_f32 %1, %2, %1\n\tv_accvgpr_write_b32 a%c5, %0\n\tv_accvgpr_write_b32 a%c6, %1"
               : "=&v"(t0), "=&v"(t1) : "v"(f), "i"(A), "i"(A + 1), "i"(A + 2), "i"(A + 3)); }
DI void own_acc() { asm volatile("" ::: ACLOB); }
DI unsigned cvtpk(float lo, float hi) { f32x2 v = {lo, hi}; bf16x2 b = __builtin_convertvector(v, bf16x2); return __builtin_bit_cast(unsigned, b); }
DI bf16x8 pack8(f32x4 a, f32x4 b) { u32x4 w = {cvtpk(a[0], a[1]), cvtpk(a[2], a[3]), cvtpk(b[0], b[1]), cvtpk(b[2], b[3])}; return __builtin_bit_cast(bf16x8, w); }
DI float max3f(float a, float b, float c) { float r; asm("v_max3_f32 %0, %1, %2, %3" : "=v"(r) : "v"(a), "v"(b), "v"(c)); return r; }
DI float xmax16_32(float v) {
  auto r = __builtin_amdgcn_permlane16_swap(__float_as_uint(v), __float_as_uint(v), false, false);
  v = fmaxf(__uint_as_float(r[0]), __uint_as_float(r[1]));
  auto r2 = __builtin_amdgcn_permlane32_swap(__float_as_uint(v), __float_as_uint(v), false, false);
  return fmaxf(__uint_as_float(r2[0]), __uint_as_float(r2[1]));
}

#define RST(i)
#define RSP
#define RSA

struct KV { srd_t k, v; unsigned hoff; unsigned voffK[4], voffV[2]; };
DI unsigned kv_soff(const KV& kv, int wv, int tile, bool valid) { return valid ? kv.hoff + (unsigned)(wv >> 1) * HEAD_BYTES + (unsigned)tile * 8192u + (unsigned)(wv & 1) * 4096u : OOB; }
template <int P> DI void pieceK(const KV& kv, unsigned so) { dmal<P * 1024u, true>(kv.voffK[P], kv.k, so); }
template <int P> DI void pieceV(const KV& kv, unsigned so) { dmal<P * 1024u, true>(kv.voffV[P & 1], kv.v, so); }
template <bool ISV> DI void issueKV(const KV& kv, unsigned ldsb, int wv, int tile, unsigned slot) {
  unsigned so = kv_soff(kv, wv, tile, true), d = ldsb + slot * UNIT + (unsigned)wv * 4096u;
  setm0<0>(d); snop0();
  sfor<4>([&](auto I) INL { if constexpr (ISV) pieceV<I>(kv, so); else pieceK<I>(kv, so); });
}

template <int ROLE>
DI void qphase(RSP unsigned ldsb, int lane, srd_t srdW, srd_t srdX, const KV& kv, unsigned wcol, unsigned xrow0, int rot, char* fws) {
  constexpr int w = ROLE, hh = w >> 1, rh = w & 1;
  const int g = lane >> 4, q = (lane >> 2) & 3, p = lane & 3, r16 = lane & 15;
  unsigned vo[4];
  if constexpr (ROLE == 0) sfor<4>([&](auto I) INL { constexpr int i = I; vo[i] = (lane >> 4) * 8192u + (((lane & 15) ^ ((lane >> 4) << 2)) * 16u) + i * 32768u - ((i >> 1) * 2056u + (i & 1) * 1024u); });
  if constexpr (ROLE == 1) sfor<4>([&](auto I) INL { constexpr int i = I; vo[i] = (lane >> 3) * 1152u + (((lane & 7) ^ ((4 * (i & 1) + (lane >> 4)) & 7)) * 16u) + i * (9216u - 1024u); });
  constexpr unsigned WOFF[4] = {0u, 1024u, 2056u, 3080u};
  auto rotp = [&](int pp) INL { int pair = pp + rot; return pair >= 9 ? pair - 9 : pair; };
  auto soW = [&](int pos) INL { return pos < 18 ? (unsigned)(2 * rotp(pos >> 1) + (pos & 1)) * (32u * 8192u) + wcol : OOB; };
  auto soX = [&](int pp) INL { return pp < 9 ? xrow0 * 1152u + (unsigned)rotp(pp) * 128u : OOB; };
  auto setW = [&](auto BUF, auto G) INL { setm0<WQB + BUF * WQ_BUF + (G >> 1) * WQ_HEAD + (G & 1) * 4112u>(ldsb); };
  auto setX = [&](auto BUF, auto G) INL { setm0<XB + BUF * X_BUF + G * 4096u>(ldsb); };
  auto offW = [](int gI) { return (unsigned)(gI >> 1) * 256u + (unsigned)(gI & 1) * 131072u; };
  unsigned wb[4], xb[2];
#pragma unroll
  for (int s = 0; s < 4; ++s) wb[s] = ldsb + WQB + hh * WQ_HEAD + g * 2056u + q * 256u + ((s ^ q) * 64u) + p * 16u;
#pragma unroll
  for (int k2 = 0; k2 < 2; ++k2) xb[k2] = ldsb + XB + rh * 10240u + r16 * 128u + (((4 * k2 + g) ^ (r16 >> 1)) * 16u);
  if constexpr (ROLE == 0) sfor<3>([&](auto B) INL { const unsigned so = soW(B);
    sfor<4>([&](auto G) INL { unsigned s2 = so + offW(G); pin(s2); setW(B, G); snop0(); sfor<4>([&](auto I) INL { dmal<WOFF[I], WQ_NT>(vo[I], srdW, s2); }); }); });
  if constexpr (ROLE == 1) sfor<3>([&](auto B) INL { const unsigned so = soX(B);
    sfor<5>([&](auto G) INL { unsigned s2 = so + G * 36864u; pin(s2); setX(B, G); snop0(); sfor<4>([&](auto I) INL { dmal<I * 1024u, X_NT>(vo[I], srdX, s2); }); }); });
  sfor<40>([&](auto I) INL { azero<4 * I>(); });
  if constexpr (ROLE == 0) wait_bar<32>(); else if constexpr (ROLE == 1) wait_bar<40>(); else bar_only();
  RST(4);
  bf16x4 alo[2][8], ahi[2][8]; bf16x8 bfr[2][5];
  sfor<8>([&](auto T) INL { constexpr int t = T; alo[0][t] = ldstr1(wb[t >> 1] + (t & 1) * 8u); ahi[0][t] = ldstr1(wb[t >> 1] + (t & 1) * 8u + 1024u); });
  sfor<5>([&](auto T) INL { bfr[0][T] = lds128(xb[0] + T * 2048u); });
  unsigned soN = 0;
  if constexpr (ROLE == 0) soN = soW(3);
  if constexpr (ROLE == 1) soN = OOB;
#define FT(i)
  auto six = [&](auto IT) INL {
    const int it = IT;
    sfor<6>([&](auto J) INL {
      constexpr int j = J, k2 = j & 1, cur = j & 1, nxt = cur ^ 1;
      constexpr int wnb = (j + 1) % 3;
      constexpr int xnb = ((j + 1) >> 1) % 3;
      const int pos = 6 * it + j, pp = pos >> 1;
      if constexpr (ROLE == 0) wait_bar<16>();
      else if constexpr (ROLE == 1) { if constexpr (k2 == 1) wait_bar<20>(); else bar_only(); }
      else bar_only();
      unsigned so = soN, s2 = 0;
      sfor<40>([&](auto M) INL {
        constexpr int m = M, t = m / 5, qt = m % 5;
        if constexpr (m == 8) FT(1);
        if constexpr (m == 16) FT(2);
        if constexpr (m == 24) FT(3);
        if constexpr (m == 32) FT(4);
        mfa<AO(t, qt)>(cat(alo[cur][t], ahi[cur][t]), bfr[cur][qt]);
        GAP();
        if constexpr (m < 5) bfr[nxt][m] = lds128(xb[k2 ^ 1] + xnb * X_BUF + m * 2048u);
        else if constexpr (m < 21) { constexpr int i = m - 5, t2 = i >> 1;
          if constexpr ((i & 1) == 0) alo[nxt][t2] = ldstr1(wb[t2 >> 1] + wnb * WQ_BUF + (t2 & 1) * 8u);
          else ahi[nxt][t2] = ldstr1(wb[t2 >> 1] + wnb * WQ_BUF + (t2 & 1) * 8u + 1024u); }
        if constexpr (ROLE == 0) {
          if constexpr (m % 5 == 0 && m / 5 < 4) { s2 = so + offW(m / 5); pin(s2); setW(IC<j % 3>{}, IC<m / 5>{}); }
          if constexpr (m % 5 != 0 && m / 5 < 4) dmal<WOFF[m % 5 - 1], WQ_NT>(vo[m % 5 - 1], srdW, s2);
          if constexpr (m == 38) { soN = soW(pos + 4); pin(soN); }
        }
        if constexpr (ROLE == 1) {
          constexpr int g0 = k2 == 1 ? 0 : 3, ng = k2 == 1 ? 3 : 2, xbuf = k2 == 1 ? (j >> 1) % 3 : ((j >> 1) + 2) % 3, mm = m - 20;
          if constexpr (m >= 20 && mm % 6 == 0 && mm / 6 < ng) { s2 = so + (g0 + mm / 6) * 36864u; pin(s2); setX(IC<xbuf>{}, IC<g0 + mm / 6>{}); }
          if constexpr (m >= 20 && mm % 6 >= 1 && mm % 6 <= 4 && mm / 6 < ng) { if (j != 0 || it != 0) dmal<(mm % 6 - 1) * 1024u, X_NT>(vo[mm % 6 - 1], srdX, s2); }
          if constexpr (m == 39 && k2 == 0) { soN = soX(pp + 3); pin(soN); }
        }
        if constexpr (ROLE >= 2) {
          if constexpr (m == 22) {
            if (pos < 6) { const int u = pos >> 1, wv = 2 * (pos & 1) + (ROLE - 2); s2 = kv_soff(kv, wv, u == 1 ? 1 : 0, true); pin(s2); setm0<0>(ldsb + (unsigned)u * UNIT + (unsigned)wv * 4096u); }
            else { const int i = pos - 6;
              s2 = i < 4 ? kv_soff(kv, i, ROLE == 2 ? 2 : 1, true) : kv_soff(kv, i - 4 + 2 * (ROLE - 2), 3, pos < 12); pin(s2); }
          }
          if constexpr (m >= 24 && m <= 36 && (m - 24) % 4 == 0) { constexpr int pc = (m - 24) / 4;
            if (it == 0) { if (pos >= 4) pieceV<pc>(kv, s2); else pieceK<pc>(kv, s2); }
            else if (it == 1) { constexpr int A = 160 + 16 * j + 4 * pc;
              if constexpr (ROLE == 3 && j < 4) loada<A, pc * 1024u>(kv.voffV[pc & 1], kv.v, s2); else loada<A, pc * 1024u>(kv.voffK[pc], kv.k, s2); }
          }
        }
        GAP();
      });
    });
  };
  if constexpr (ROLE >= 2) sfor<3>([&](auto IT) INL { six(IT); });
  else for (int it = 0; it < 3; ++it) six(it);
}

__global__ __launch_bounds__(256, 1) void attn_kernel(const char* xA, const char* kA, const char* vA, const char* wqA,
                                                      const char* xB_, const char* kB, const char* vB, const char* wqB, char* of) {
  extern __shared__ __attribute__((aligned(16))) char lds[];
  const unsigned ldsb = (unsigned)(size_t)(AS3 char*)lds;
  const int tid = threadIdx.x, lane = tid & 63, w = __builtin_amdgcn_readfirstlane(tid >> 6);
  const int bid = blockIdx.x, j = bid & 7, br = j >> 2, hg = j & 3, b = bid >> 5, hp = (bid >> 3) & 3;
  const int h0 = hg * 8 + hp * 2, hh = w >> 1, rh = w & 1;
  const int g = lane >> 4, q = (lane >> 2) & 3, p = lane & 3, r16 = lane & 15;
  srd_t srdX = mksrd(br ? xB_ : xA, X_BYTES), srdW = mksrd(br ? wqB : wqA, W_BYTES);
  KV kv; kv.k = mksrd(br ? kB : kA, KV_BYTES); kv.v = mksrd(br ? vB : vA, KV_BYTES);
  kv.hoff = (unsigned)(b * NH + h0) * HEAD_BYTES;
#pragma unroll
  for (int pc = 0; pc < 4; ++pc) kv.voffK[pc] = (lane >> 4) * 256u + (((lane & 15) ^ (4 * pc + (lane >> 4))) * 16u);
#pragma unroll
  for (int pc = 0; pc < 2; ++pc) kv.voffV[pc] = (lane >> 4) * 256u + (((lane & 15) ^ (((4 * pc + (lane >> 4)) & 7) << 1)) * 16u);

  own_acc();
  {
    const int rot = ROT;
    if (w == 0) qphase<0>(RSA ldsb, lane, srdW, srdX, kv, (unsigned)h0 * 256u, (unsigned)b * SQ, rot, of);
    else if (w == 1) qphase<1>(RSA ldsb, lane, srdW, srdX, kv, (unsigned)h0 * 256u, (unsigned)b * SQ, rot, of);
    else if (w == 2) qphase<2>(RSA ldsb, lane, srdW, srdX, kv, (unsigned)h0 * 256u, (unsigned)b * SQ, rot, of);
    else qphase<3>(RSA ldsb, lane, srdW, srdX, kv, (unsigned)h0 * 256u, (unsigned)b * SQ, rot, of);
    wait_bar<0>();
    RST(1);
    issueKV<true>(kv, ldsb, w, 2, 6); issueKV<false>(kv, ldsb, w, 4, 7); issueKV<true>(kv, ldsb, w, 3, 8); issueKV<false>(kv, ldsb, w, 5, 9);
    if (w >= 2) {
      const unsigned da = ldsb + (unsigned)(w + 1) * UNIT + lane * 16u, db = ldsb + 5 * UNIT + (unsigned)(w - 2) * 8192u + lane * 16u;
      sfor<16>([&](auto I) INL { dswa<160 + 4 * I, I * 1024u>(da); });
      sfor<8>([&](auto I) INL { dswa<224 + 4 * I, I * 1024u>(db); });
      wait_lgkm0();
    }
    GAP();
    asm volatile("s_nop 7" ::: "memory");
    sfor<20>([&](auto I) INL { constexpr int qt = I / 4, s = I % 4;
      awrite<AQ(qt, s)>(__builtin_bit_cast(u32x4, pack8(aread<AO(2 * s, qt)>() * QSCALE, aread<AO(2 * s + 1, qt)>() * QSCALE))); });
    awrite<AONES>(u32x4{0x3F803F80u, 0x3F803F80u, 0x3F803F80u, 0x3F803F80u});
    azero<AL0>(); azero<AL0 + 4>(); azero<AL0 + 8>();
    GAP();
  }
  unsigned ka[4], va[8];
#pragma unroll
  for (int s = 0; s < 4; ++s) ka[s] = ldsb + hh * 8192u + r16 * 256u + (((4 * s + g) ^ r16) * 16u);
#pragma unroll
  for (int dt = 0; dt < 8; ++dt) va[dt] = ldsb + 2 * UNIT + hh * 8192u + (4 * g + q) * 256u + ((dt ^ ((4 * g + q) & 7)) * 32u) + p * 8u;

  f32x4 L3 = {0, 0, 0, 0}, L4 = {0, 0, 0, 0}, negm[5], S0[2][5], S1[2][5];
  bf16x8 kf[2][4];
  sfor<8>([&](auto I) INL { kf[I / 4][I % 4] = lds128(ka[I % 4] + (I / 4) * 4096u); });
  GAP();
  asm volatile("s_nop 1" ::: "memory");
  sfor<40>([&](auto M) INL { constexpr int m = M, tile = m >> 2, s = m & 3, kt = tile / 5, qt = tile % 5;
    if constexpr (s == 0) mfsz<AQ(qt, 0)>(S0[kt][qt], kf[kt][0]); else mfs<AQ(qt, s)>(S0[kt][qt], kf[kt][s]); });
  GAP();
  sfor<8>([&](auto I) INL { kf[I / 4][I % 4] = lds128(ka[I % 4] + UNIT + (I / 4) * 4096u); });
  sfor<4>([&](auto I) INL { ka[I] += 3 * UNIT; });
  nop16();
  sfor<5>([&](auto QT) INL { constexpr int qt = QT;
    float m = max3f(max3f(S0[0][qt][0], S0[0][qt][1], S0[0][qt][2]), max3f(S0[0][qt][3], S0[1][qt][0], S0[1][qt][1]), fmaxf(S0[1][qt][2], S0[1][qt][3]));
    m = xmax16_32(m);
    negm[qt] = f32x4{-m, -m, -m, -m};
    S0[0][qt] -= m; S0[1][qt] -= m; });
  GAP();
  RST(7);
  unsigned slotF = 0, slotV = 2;
  unsigned dF = ldsb + (unsigned)w * 4096u, soV = kv_soff(kv, w, 4, true), soK = kv_soff(kv, w, 6, true), adv = 2 * UNIT;
  auto iter = [&](int t, f32x4 (&Sc)[2][5], f32x4 (&Sn)[2][5], auto do_qk_, auto do_k_, auto first_) INL {
    constexpr bool do_qk = decltype(do_qk_)::value, do_k = decltype(do_k_)::value, first = decltype(first_)::value;
    wait_bar<24>();
    own_acc();
    u32x4 Pw[5]; float ex[40]; bf16x4 vlo[8], vhi[8];
    GAP();
    sfor<40>([&](auto M) INL {
      constexpr int m = M, tile = m >> 2, s = m & 3, kt = tile / 5, qt = tile % 5;
      if constexpr (do_qk) { if constexpr (s == 0) mfs0<AQ(qt, 0)>(Sn[kt][qt], kf[kt][0], negm[qt]); else mfs<AQ(qt, s)>(Sn[kt][qt], kf[kt][s]); }
      GAP();
      ex[m] = __builtin_amdgcn_exp2f(Sc[(m & 7) >> 2][m >> 3][m & 3]);
      if constexpr (m >= 2 && (m & 1) == 0) Pw[(m - 2) >> 3][((m - 2) & 7) >> 1] = cvtpk(ex[m - 2], ex[m - 1]);
      if constexpr (m == 33) vlo[0] = ldstr1(va[0]);
      if constexpr (m == 35) vhi[0] = ldstr1(va[0] + 4096u);
      if constexpr (m == 37) vlo[1] = ldstr1(va[1]);
      if constexpr (m == 39) vhi[1] = ldstr1(va[1] + 4096u);
      GAP();
    });
    Pw[4][3] = cvtpk(ex[38], ex[39]);
    float mx0 = 0.f, mx1 = 0.f;
    const unsigned advc = adv;
    unsigned dK = 0;
    GAP();
    sfor<45>([&](auto M) INL {
      constexpr int m = M;
      if constexpr (m < 40) { constexpr int dt = m / 5, qt = m % 5;
        if constexpr (first) mfa0<AO(dt, qt)>(cat(vlo[dt], vhi[dt]), __builtin_bit_cast(bf16x8, Pw[qt]));
        else mfa<AO(dt, qt)>(cat(vlo[dt], vhi[dt]), __builtin_bit_cast(bf16x8, Pw[qt])); }
      else if constexpr (m < 43) mfl<AL0 + 4 * (m - 40)>(__builtin_bit_cast(bf16x8, Pw[m - 40]));
      else if constexpr (m == 43) mflv(L3, __builtin_bit_cast(bf16x8, Pw[3]));
      else mflv(L4, __builtin_bit_cast(bf16x8, Pw[4]));
      GAP();
      if constexpr (m < 30 && m % 5 == 0) vlo[m / 5 + 2] = ldstr1(va[m / 5 + 2]);
      if constexpr (m < 30 && m % 5 == 1) vhi[m / 5 + 2] = ldstr1(va[m / 5 + 2] + 4096u);
      if constexpr (do_k && m < 40 && m % 5 == 2) { constexpr int i = m / 5; kf[i >> 2][i & 3] = lds128(ka[i & 3] + (i >> 2) * 4096u); }
      if constexpr (m == 0) setm0<0>(dF);
      if constexpr (m == 19) { dK = dF + UNIT; pin(dK); }
      if constexpr (m == 20) setm0<0>(dK);
      if constexpr (m < 40 && m % 5 == 3) { constexpr int n = m / 5; if constexpr (n < 4) pieceV<n>(kv, soV); else pieceK<n - 4>(kv, soK); }
      if constexpr (m >= 27 && m < 35) { va[m - 27] += advc; }
      if constexpr (do_k && m >= 40 && m < 44) { ka[m - 40] += advc; }
      if constexpr (do_qk && m >= 6 && m < 26) { constexpr int i = m - 6, tile = i >> 1, kt = tile / 5, qt = tile % 5;
        if constexpr (i == 0) mx0 = max3f(Sn[0][0][0], Sn[0][0][1], Sn[0][0][1]);
        else if constexpr (i == 1) mx1 = max3f(Sn[0][0][2], Sn[0][0][3], Sn[0][0][3]);
        else if constexpr (i & 1) mx1 = max3f(mx1, Sn[kt][qt][2], Sn[kt][qt][3]);
        else mx0 = max3f(mx0, Sn[kt][qt][0], Sn[kt][qt][1]); }
      if constexpr (m == 39) { slotF = slotF == 8 ? 0 : slotF + 2; pin(slotF); }
      if constexpr (m == 40) { dF = ldsb + slotF * UNIT + (unsigned)w * 4096u; pin(dF); }
      if constexpr (m == 41) { soV = t + 5 < 16 ? soV + 8192u : OOB; pin(soV); }
      if constexpr (m == 42) { soK = t + 7 < 16 ? soK + 8192u : OOB; pin(soK); }
      if constexpr (m == 43) { slotV = slotV == 8 ? 0 : slotV + 2; pin(slotV); }
      if constexpr (m == 44) { adv = slotV == 8 ? (unsigned)(-8 * (int)UNIT) : 2 * UNIT; pin(adv); }
      GAP();
    });
    if constexpr (do_qk) {
      if (__builtin_expect(__any(!(fmaxf(mx0, mx1) <= THR)), 0)) {
        nop16();
        sfor<5>([&](auto QT) INL { constexpr int qt = QT;
          float m = max3f(max3f(Sn[0][qt][0], Sn[0][qt][1], Sn[0][qt][2]), max3f(Sn[0][qt][3], Sn[1][qt][0], Sn[1][qt][1]), fmaxf(Sn[1][qt][2], Sn[1][qt][3]));
          m = fmaxf(xmax16_32(m), 0.f);
          const float f = __builtin_amdgcn_exp2f(-m);
          negm[qt] -= m; Sn[0][qt] -= m; Sn[1][qt] -= m;
          if constexpr (qt < 3) ascale<AL0 + 4 * qt>(f); else if constexpr (qt == 3) L3 *= f; else L4 *= f;
          sfor<8>([&](auto DT) INL { ascale<AO(DT, qt)>(f); }); });
        asm volatile("s_nop 1" ::: "memory");
        GAP();
      }
    }
  };
  using T = std::true_type; using F = std::false_type;
  iter(0, S0, S1, T{}, T{}, T{});
  for (int t = 1; t < 13; t += 2) { iter(t, S1, S0, T{}, T{}, F{}); iter(t + 1, S0, S1, T{}, T{}, F{}); }
  iter(13, S1, S0, T{}, T{}, F{});
  iter(14, S0, S1, T{}, F{}, F{});
  iter(15, S1, S0, F{}, F{}, F{});
  nop16();
  RST(2);
  char* ob = of + ((size_t)((br * 8 + b) * 32 + h0 + hh) * 40 + 5 * rh) * 1024 + lane * 16;
  sfor<5>([&](auto QT) INL { constexpr int qt = QT;
    float l; if constexpr (qt < 3) l = aread<AL0 + 4 * qt>()[0]; else if constexpr (qt == 3) l = L3[0]; else l = L4[0];
    const float inv = 1.0f / l;
    sfor<4>([&](auto S) INL { constexpr int s = S;
      *(bf16x8*)(ob + (s * 10 + qt) * 1024) = pack8(aread<AO(2 * s, qt)>() * inv, aread<AO(2 * s + 1, qt)>() * inv); }); });
  wait_vm<0>();
}

constexpr unsigned ST_BYTES = 38912, ST_WO = 20480;
template <int NHF>
DI void oproj(unsigned ldsb, int w, int lane, srd_t srdO, srd_t srdWo, unsigned obase, unsigned wobase, int rot, char* part, char* ws) {
  const int rh = w >> 1, g = lane >> 4, q = (lane >> 2) & 3, p = lane & 3;
  const unsigned wpi0 = (w < 2 ? 5 * w : 10 + 4 * (w - 2));
  unsigned voffWo[5];
#pragma unroll
  for (int n = 0; n < 5; ++n) { int c = 64 * (wpi0 + n) + lane; voffWo[n] = (c / 18) * 1152u + (c % 18) * 16u - (n & 3) * 1024u; }
  const unsigned voffO = lane * 16u;
  const bool five = w < 2;
  auto soO = [&](int pos) INL { return pos < 16 ? obase + (unsigned)((pos + rot) & 15) * 20480u + (unsigned)w * 5120u : OOB; };
  auto soWo = [&](int pos) INL { return pos < 16 ? wobase + (unsigned)((pos + rot) & 15) * (64u * 1152u) : OOB; };
  const unsigned dO0 = ldsb + (unsigned)w * 5120u, dW0 = ldsb + ST_WO + wpi0 * 1024u;
  const unsigned ab = ldsb + ST_WO + (4 * g + q) * 288u + p * 8u, bb = ldsb + rh * 5120u + lane * 16u;
  own_acc();
  auto setup = [&](auto N, auto BUF, unsigned so, unsigned sw, unsigned& s2) INL { constexpr int n = N;
    if constexpr (n == 0) { setm0<BUF * ST_BYTES>(dO0); s2 = so; }
    if constexpr (n == 4) { setm0<BUF * ST_BYTES + 4096u>(dO0); s2 = so + 4096u; }
    if constexpr (n == 5) { setm0<BUF * ST_BYTES>(dW0); s2 = sw; }
    if constexpr (n == 9) { setm0<0>(five ? dW0 + BUF * ST_BYTES + 4096u : ldsb + 4 * ST_BYTES); s2 = five ? sw : OOB; }
    pin(s2); };
  auto load = [&](auto N, unsigned s2) INL { constexpr int n = N;
    if constexpr (n < 4) dmal<n * 1024u, false>(voffO, srdO, s2);
    else if constexpr (n == 4) dmal<0, false>(voffO, srdO, s2);
    else if constexpr (n < 9) dmal<(n - 5) * 1024u, true>(voffWo[n - 5], srdWo, s2);
    else dmal<0, true>(voffWo[4], srdWo, s2); };
  sfor<3>([&](auto S) INL { unsigned so = soO(S), sw = soWo(S), s2 = 0;
    sfor<10>([&](auto N) INL { constexpr int n = N; if constexpr (n == 0 || n == 4 || n == 5 || n == 9) { setup(N, S, so, sw, s2); snop0(); } load(N, s2); }); });
  sfor<25>([&](auto I) INL { azero<4 * I>(); });
  wait_bar<20>();
  RST(1);
  bf16x4 alo[2][5], ahi[2][5]; bf16x8 bfr[2][5];
  auto ntof = [](int a) { return a == 4 ? 4 : NHF * 5 + a; };
  sfor<5>([&](auto A) INL { constexpr int a = A; if constexpr (a < 4 || NHF == 0) { alo[0][a] = ldstr1(ab + ntof(a) * 32u); ahi[0][a] = ldstr1(ab + ntof(a) * 32u + 4608u); } });
  sfor<5>([&](auto T) INL { bfr[0][T] = lds128(bb + T * 1024u); });
  unsigned soN = soO(3), swN = soWo(3);
  for (int it = 0; it < 4; ++it) {
    sfor<4>([&](auto J) INL {
      constexpr int jb = J, nb = (jb + 1) & 3, db = (jb + 3) & 3;
      const int pos = 4 * it + jb;
      wait_bar<10>();
      const unsigned so = soN, sw = swN; unsigned s2 = 0;
      sfor<2>([&](auto K2) INL {
        constexpr int k2 = K2, na = (k2 == NHF) ? 5 : 4, nan = ((k2 ^ 1) == NHF) ? 5 : 4;
        constexpr unsigned rb = k2 == 0 ? jb * ST_BYTES : nb * ST_BYTES, hi = rb >= 2 * ST_BYTES ? 2 * ST_BYTES : (rb >= ST_BYTES ? ST_BYTES : 0);
        constexpr unsigned aoff = (k2 == 0 ? 9216u : 0u), boff = (k2 == 0 ? 10240u : 0u);
        const unsigned ar = ab + rb, brd = bb + rb; (void)hi;
        sfor<na * 5>([&](auto M) INL {
          constexpr int m = M, a = m / 5, qt = m % 5;
          mfa<AO(a, qt)>(cat(alo[k2][a], ahi[k2][a]), bfr[k2][qt]);
          GAP();
          if constexpr (m < 5) bfr[k2 ^ 1][m] = lds128(brd + boff + m * 1024u);
          else if constexpr (m < 5 + 2 * nan) { constexpr int i = m - 5, a2 = i >> 1;
            if constexpr ((i & 1) == 0) alo[k2 ^ 1][a2] = ldstr1(ar + aoff + ntof(a2) * 32u); else ahi[k2 ^ 1][a2] = ldstr1(ar + aoff + ntof(a2) * 32u + 4608u); }
          if constexpr (m % 4 == 0 && m / 4 < 5) { constexpr int n = 5 * k2 + m / 4; if constexpr (n == 0 || n == 4 || n == 5 || n == 9) setup(IC<n>{}, IC<db>{}, so, sw, s2); }
          if constexpr (m % 4 == 2 && m / 4 < 5) load(IC<5 * k2 + m / 4>{}, s2);
          if constexpr (k2 == 1 && m == 19) { soN = soO(pos + 4); swN = soWo(pos + 4); pin(soN); pin(swN); }
          GAP();
        });
      });
    });
  }
  nop16();
  RST(2);
  sfor<25>([&](auto I) INL { constexpr int a = I / 5, qt = I % 5, slot = a == 4 ? 4 + NHF : (NHF ? 6 + a : a);
    *(f32x4*)(part + (slot * 10 + 5 * rh + qt) * 1024 + lane * 16) = aread<AO(a, qt)>(); });
  wait_vm<0>();
}
__global__ __launch_bounds__(256, 1) void oproj_kernel(const char* of, const char* woA, const char* woB, char* parts) {
  extern __shared__ __attribute__((aligned(16))) char lds[];
  const unsigned ldsb = (unsigned)(size_t)(AS3 char*)lds;
  const int tid = threadIdx.x, lane = tid & 63, w = __builtin_amdgcn_readfirstlane(tid >> 6);
  const int bid = blockIdx.x, j = bid & 7, br = j >> 2, hg = j & 3, b = bid >> 5, nq = (bid >> 3) & 3;
  srd_t srdO = mksrd(of, (unsigned)OF_BYTES), srdWo = mksrd(br ? woB : woA, W_BYTES);
  const unsigned obase = (unsigned)((br * 8 + b) * 32 + hg * 8) * 40960u, wobase = (unsigned)hg * 1024u * 1152u + nq * 288u;
  char* part = parts + (size_t)(((br * 4 + hg) * 8 + b) * 4 + nq) * 102400;
  const int rot = (2 * b + 4 * nq) & 15;
  if (w & 1) oproj<1>(ldsb, w, lane, srdO, srdWo, obase, wobase, rot, part, (char*)of);
  else oproj<0>(ldsb, w, lane, srdO, srdWo, obase, wobase, rot, part, (char*)of);
}
__global__ __launch_bounds__(256) void reduce_kernel(const char* parts, unsigned short* out) {
  const int lane = threadIdx.x & 63, tile = blockIdx.x * 4 + (threadIdx.x >> 6);
  const int qt = tile % 10, nt = (tile / 10) % 9, rest = tile / 90, nq = rest & 3, b = (rest >> 2) & 7, br = rest >> 5;
  f32x4 s = {0, 0, 0, 0};
#pragma unroll
  for (int hg = 0; hg < 4; ++hg) {
    const char* part = parts + (size_t)(((br * 4 + hg) * 8 + b) * 4 + nq) * 102400 + lane * 16;
    const int slot = nt < 4 ? nt : (nt == 4 ? 4 : nt + 1);
    s += *(const f32x4*)(part + (slot * 10 + qt) * 1024);
    if (nt == 4) s += *(const f32x4*)(part + (5 * 10 + qt) * 1024);
  }
  const int row = b * SQ + qt * 16 + (lane & 15), col = br * 576 + nq * 144 + nt * 16 + (lane >> 4) * 4;
  u32x2 o = {cvtpk(s[0], s[1]), cvtpk(s[2], s[3])};
  *(u32x2*)(out + (size_t)row * 1152 + col) = o;
}

extern "C" void kernel_launch(void* const* d_in, const int* in_sizes, int n_in, void* d_out, int out_size, void* d_ws, size_t ws_size, hipStream_t stream) {
  char* ws = (char*)d_ws;
  (void)hipFuncSetAttribute((const void*)attn_kernel, hipFuncAttributeMaxDynamicSharedMemorySize, 163840);
  (void)hipFuncSetAttribute((const void*)oproj_kernel, hipFuncAttributeMaxDynamicSharedMemorySize, 163840);
  attn_kernel<<<256, 256, 163840, stream>>>((const char*)d_in[0], (const char*)d_in[1], (const char*)d_in[2], (const char*)d_in[3],
                                            (const char*)d_in[5], (const char*)d_in[6], (const char*)d_in[7], (const char*)d_in[8], ws + WS_OF);
  oproj_kernel<<<256, 256, 163840, stream>>>(ws + WS_OF, (const char*)d_in[4], (const char*)d_in[9], ws + WS_PART);
  reduce_kernel<<<2 * 8 * 4 * 90 / 4, 256, 0, stream>>>(ws + WS_PART, (unsigned short*)d_out);
}
```

```cpp
#include <hip/hip_runtime.h>
#include <hip/hip_bf16.h>
#include <type_traits>
#include <utility>

typedef __bf16 bf16x8 __attribute__((ext_vector_type(8)));
typedef __bf16 bf16x4 __attribute__((ext_vector_type(4)));
typedef __bf16 bf16x2 __attribute__((ext_vector_type(2)));
typedef float f32x4 __attribute__((ext_vector_type(4)));
typedef float f32x2 __attribute__((ext_vector_type(2)));
typedef unsigned u32x4 __attribute__((ext_vector_type(4)));
typedef unsigned u32x2 __attribute__((ext_vector_type(2)));
#define AS3 __attribute__((address_space(3)))
#define DI __device__ __forceinline__
#define INL __attribute__((always_inline))
#define GAP() __builtin_amdgcn_sched_barrier(0)
typedef __amdgpu_buffer_rsrc_t srd_t;
template <class F, int... Is> DI void sfor_(F&& f, std::integer_sequence<int, Is...>) { (f(std::integral_constant<int, Is>{}), ...); }
template <int N, class F> DI void sfor(F&& f) { sfor_(f, std::make_integer_sequence<int, N>{}); }
template <int V> using IC = std::integral_constant<int, V>;

constexpr int SQ = 160, DM = 576, NH = 32, SK = 512, DH = 128, NB = 8;
constexpr unsigned KV_BYTES = (unsigned)NB * NH * SK * DH * 2;
constexpr unsigned W_BYTES = DM * 4096 * 2;
constexpr unsigned X_BYTES = NB * SQ * DM * 2;
constexpr unsigned HEAD_BYTES = SK * DH * 2;
constexpr unsigned OOB = 0x40000000u;
constexpr float QSCALE = 0.08838834764831845f * 1.4426950408889634f;
constexpr float THR = 10.0f;
#define WQ_NT false
#define X_NT false
#define ROT ((b + 2 * hp + hp / 2) % 9)
#define NP 5
#define PSTEPS {2, 5, 8, 11, 13, 1}
#define OROT (((bid >> 5) * 3) >> 2)
#define KVB 1
#define KVROT ((bid * 5 + (bid >> 4)) & 15)
#define CODEWARM 1
#define WO_NT false
#define O_POL "sc1"
#define TOUCH_POL ""

constexpr size_t WS_OF = 0;
constexpr size_t OF_BYTES = (size_t)2 * 8 * 32 * 4 * 10 * 1024;
constexpr size_t WS_PART = WS_OF + OF_BYTES;
constexpr size_t PART_BYTES = (size_t)256 * 4 * 4 * 4 * 1024;
constexpr size_t WS_FLAG = WS_PART + PART_BYTES;

constexpr unsigned UNIT = 16384;
constexpr unsigned WQB = 3 * UNIT, WQ_BUF = 16448, WQ_HEAD = 8224;
constexpr unsigned XB = WQB + 3 * WQ_BUF, X_BUF = 20480;
static_assert(XB + 3 * X_BUF <= 163840, "lds");

DI srd_t mksrd(const void* p, unsigned bytes) { return __builtin_amdgcn_make_buffer_rsrc((void*)p, (short)0, (int)bytes, 0x00020000); }
template <unsigned IMM> DI void setm0(unsigned base) { asm volatile("s_add_u32 m0, %0, %c1" :: "s"(base), "i"(IMM) : "scc"); }
template <unsigned OFF, bool NT> DI void dmal(unsigned voff, srd_t srd, unsigned soff) {
  if constexpr (NT) asm volatile("buffer_load_dwordx4 %0, %1, %2 offen offset:%c3 nt lds" :: "v"(voff), "s"(srd), "s"(soff), "i"(OFF) : "memory");
  else              asm volatile("buffer_load_dwordx4 %0, %1, %2 offen offset:%c3 lds" :: "v"(voff), "s"(srd), "s"(soff), "i"(OFF) : "memory");
}
template <unsigned OFF> DI void dmal_sc1(unsigned voff, srd_t srd, unsigned soff) {
  asm volatile("buffer_load_dwordx4 %0, %1, %2 offen offset:%c3 " O_POL " lds" :: "v"(voff), "s"(srd), "s"(soff), "i"(OFF) : "memory"); }
template <int A, unsigned OFF> DI void loada(unsigned voff, srd_t srd, unsigned soff) {
  asm volatile("buffer_load_dwordx4 a[%c3:%c4], %0, %1, %2 offen offset:%c5 nt" :: "v"(voff), "s"(srd), "s"(soff), "i"(A), "i"(A + 3), "i"(OFF) : "memory"); }
template <int A, unsigned OFF> DI void dswa(unsigned addr) { asm volatile("ds_write_b128 %0, a[%c1:%c2] offset:%c3" :: "v"(addr), "i"(A), "i"(A + 3), "i"(OFF) : "memory"); }
DI void touch(unsigned& junk, unsigned voff, srd_t srd, unsigned soff) { asm volatile("buffer_load_dword %0, %1, %2, %3 offen " TOUCH_POL : "+v"(junk) : "v"(voff), "s"(srd), "s"(soff) : "memory"); }
DI void store_sc1(u32x4 v, unsigned voff, srd_t srd, unsigned soff) { asm volatile("buffer_store_dwordx4 %0, %1, %2, %3 offen sc1\n\ts_nop 1" :: "v"(v), "v"(voff), "s"(srd), "s"(soff) : "memory"); }
DI void pin(unsigned& s) { asm volatile("" : "+s"(s)); }
DI void pin64(unsigned long long& s) { asm volatile("" : "+s"(s)); }
DI void pinv(unsigned& v) { asm volatile("" : "+v"(v)); }
DI void snop0() { asm volatile("s_nop 0"); }
template <int N> DI void wait_vm() { asm volatile("s_waitcnt vmcnt(%0)" :: "i"(N) : "memory"); }
DI void wait_lgkm0() { __builtin_amdgcn_s_waitcnt(0xC07F); }
template <int N> DI void wait_bar() { wait_lgkm0(); asm volatile("s_waitcnt vmcnt(%0)\n\ts_barrier" :: "i"(N) : "memory"); GAP(); }
DI void bar_only() { wait_lgkm0(); asm volatile("s_barrier" ::: "memory"); GAP(); }
DI void nop16() { asm volatile("s_nop 7\n\ts_nop 7" ::: "memory"); GAP(); }
DI bf16x8 lds128(unsigned a) { return *(const AS3 bf16x8*)(size_t)a; }
DI bf16x4 ldstr1(unsigned a) { return __builtin_amdgcn_ds_read_tr16_b64_v4bf16((AS3 bf16x4*)(size_t)a); }
DI bf16x8 cat(bf16x4 lo, bf16x4 hi) { return __builtin_shufflevector(lo, hi, 0, 1, 2, 3, 4, 5, 6, 7); }

#define ACLOB "a0","a1","a2","a3","a4","a5","a6","a7","a8","a9","a10","a11","a12","a13","a14","a15","a16","a17","a18","a19","a20","a21","a22","a23","a24","a25","a26","a27","a28","a29","a30","a31","a32","a33","a34","a35","a36","a37","a38","a39","a40","a41","a42","a43","a44","a45","a46","a47","a48","a49","a50","a51","a52","a53","a54","a55","a56","a57","a58","a59","a60","a61","a62","a63","a64","a65","a66","a67","a68","a69","a70","a71","a72","a73","a74","a75","a76","a77","a78","a79","a80","a81","a82","a83","a84","a85","a86","a87","a88","a89","a90","a91","a92","a93","a94","a95","a96","a97","a98","a99","a100","a101","a102","a103","a104","a105","a106","a107","a108","a109","a110","a111","a112","a113","a114","a115","a116","a117","a118","a119","a120","a121","a122","a123","a124","a125","a126","a127","a128","a129","a130","a131","a132","a133","a134","a135","a136","a137","a138","a139","a140","a141","a142","a143","a144","a145","a146","a147","a148","a149","a150","a151","a152","a153","a154","a155","a156","a157","a158","a159","a160","a161","a162","a163","a164","a165","a166","a167","a168","a169","a170","a171","a172","a173","a174","a175","a176","a177","a178","a179","a180","a181","a182","a183","a184","a185","a186","a187","a188","a189","a190","a191","a192","a193","a194","a195","a196","a197","a198","a199","a200","a201","a202","a203","a204","a205","a206","a207","a208","a209","a210","a211","a212","a213","a214","a215","a216","a217","a218","a219","a220","a221","a222","a223","a224","a225","a226","a227","a228","a229","a230","a231","a232","a233","a234","a235","a236","a237","a238","a239","a240","a241","a242","a243","a244","a245","a246","a247","a248","a249","a250","a251","a252","a253","a254","a255"
constexpr int AO(int t, int qt) { return (t * 5 + qt) * 4; }
constexpr int AQ(int qt, int s) { return 160 + (qt * 4 + s) * 4; }
constexpr int AONES = 240, AL0 = 244;
template <int A> DI void mfa(bf16x8 a, bf16x8 b) { asm volatile("v_mfma_f32_16x16x32_bf16 a[%c2:%c3], %0, %1, a[%c2:%c3]" :: "v"(a), "v"(b), "i"(A), "i"(A + 3)); }
template <int A> DI void mfa0(bf16x8 a, bf16x8 b) { asm volatile("v_mfma_f32_16x16x32_bf16 a[%c2:%c3], %0, %1, 0" :: "v"(a), "v"(b), "i"(A), "i"(A + 3)); }
template <int A> DI void mfl(bf16x8 b) { asm volatile("v_mfma_f32_16x16x32_bf16 a[%c1:%c2], a[%c3:%c4], %0, a[%c1:%c2]" :: "v"(b), "i"(A), "i"(A + 3), "i"(AONES), "i"(AONES + 3)); }
DI void mflv(f32x4& acc, bf16x8 b) { asm volatile("v_mfma_f32_16x16x32_bf16 %0, a[%c2:%c3], %1, %0" : "+v"(acc) : "v"(b), "i"(AONES), "i"(AONES + 3)); }
template <int A> DI void mfs0(f32x4& s, bf16x8 a, f32x4 c) { asm volatile("v_mfma_f32_16x16x32_bf16 %0, %1, a[%c3:%c4], %2" : "=&v"(s) : "v"(a), "v"(c), "i"(A), "i"(A + 3)); }
template <int A> DI void mfsz(f32x4& s, bf16x8 a) { asm volatile("v_mfma_f32_16x16x32_bf16 %0, %1, a[%c2:%c3], 0" : "=&v"(s) : "v"(a), "i"(A), "i"(A + 3)); }
template <int A> DI void mfs(f32x4& s, bf16x8 a) { asm volatile("v_mfma_f32_16x16x32_bf16 %0, %1, a[%c2:%c3], %0" : "+v"(s) : "v"(a), "i"(A), "i"(A + 3)); }
template <int A> DI f32x4 aread(void) { f32x4 r;
  asm volatile("v_accvgpr_read_b32 %0, a%c4\n\tv_accvgpr_read_b32 %1, a%c5\n\tv_accvgpr_read_b32 %2, a%c6\n\tv_accvgpr_read_b32 %3, a%c7"
               : "=v"(r[0]), "=v"(r[1]), "=v"(r[2]), "=v"(r[3]) : "i"(A), "i"(A + 1), "i"(A + 2), "i"(A + 3)); return r; }
template <int A> DI void awrite(u32x4 w) {
  asm volatile("v_accvgpr_write_b32 a%c4, %0\n\tv_accvgpr_write_b32 a%c5, %1\n\tv_accvgpr_write_b32 a%c6, %2\n\tv_accvgpr_write_b32 a%c7, %3"
               :: "v"(w[0]), "v"(w[1]), "v"(w[2]), "v"(w[3]), "i"(A), "i"(A + 1), "i"(A + 2), "i"(A + 3)); }
template <int A> DI void azero() { asm volatile("v_accvgpr_write_b32 a%c0, 0\n\tv_accvgpr_write_b32 a%c1, 0\n\tv_accvgpr_write_b32 a%c2, 0\n\tv_accvgpr_write_b32 a%c3, 0" :: "i"(A), "i"(A + 1), "i"(A + 2), "i"(A + 3)); }
template <int A> DI void ascale(float f) { float t0, t1;
  asm volatile("v_accvgpr_read_b32 %0, a%c3\n\tv_accvgpr_read_b32 %1, a%c4\n\tv_mul_f32 %0, %2, %0\n\tv_mul_f32 %1, %2, %1\n\tv_accvgpr_write_b32 a%c3, %0\n\tv_accvgpr_write_b32 a%c4, %1\n\t"
               "v_accvgpr_read_b32 %0, a%c5\n\tv_accvgpr_read_b32 %1, a%c6\n\tv_mul_f32 %0, %2, %0\n\tv_mul_f32 %1, %2, %1\n\tv_accvgpr_write_b32 a%c5, %0\n\tv_accvgpr_write_b32 a%c6, %1"
               : "=&v"(t0), "=&v"(t1) : "v"(f), "i"(A), "i"(A + 1), "i"(A + 2), "i"(A + 3)); }
DI void own_acc() { asm volatile("" ::: ACLOB); }
DI unsigned cvtpk(float lo, float hi) { f32x2 v = {lo, hi}; bf16x2 b = __builtin_convertvector(v, bf16x2); return __builtin_bit_cast(unsigned, b); }
DI bf16x8 pack8(f32x4 a, f32x4 b) { u32x4 w = {cvtpk(a[0], a[1]), cvtpk(a[2], a[3]), cvtpk(b[0], b[1]), cvtpk(b[2], b[3])}; return __builtin_bit_cast(bf16x8, w); }
#pragma float_control(precise, off, push)
DI float max3f(float a, float b, float c) { return __builtin_fmaxf(__builtin_fmaxf(a, b), c); }
DI float max2f(float a, float b) { return __builtin_fmaxf(a, b); }
#pragma float_control(pop)
DI float xmax16_32(float v) {
  auto r = __builtin_amdgcn_permlane16_swap(__float_as_uint(v), __float_as_uint(v), false, false);
  v = fmaxf(__uint_as_float(r[0]), __uint_as_float(r[1]));
  auto r2 = __builtin_amdgcn_permlane32_swap(__float_as_uint(v), __float_as_uint(v), false, false);
  return fmaxf(__uint_as_float(r2[0]), __uint_as_float(r2[1]));
}

#define RST(i)
#define RSP
#define RSA

typedef __attribute__((address_space(1))) unsigned long long gu64;
constexpr unsigned long long ACK_KEY = 0x9E3779B97F4A7C15ull;
constexpr unsigned SPIN_CAP = 1u << 20;
DI gu64* flag_word(char* ws, int wg, int idx) { return (gu64*)(ws + WS_FLAG + ((size_t)wg * 16 + idx) * 128); }
DI unsigned long long ld_agent(gu64* p) { return __hip_atomic_load(p, __ATOMIC_RELAXED, __HIP_MEMORY_SCOPE_AGENT); }
DI void st_agent(gu64* p, unsigned long long v) { __hip_atomic_store(p, v, __ATOMIC_RELAXED, __HIP_MEMORY_SCOPE_AGENT); }
DI int peer_of(int bid, int lane) { const int i = lane & 3; return (lane & 4) == 0 ? (bid & ~24) | (i << 3) : (bid & ~3) | i; }
DI int me_of(int bid, int kind) { return kind ? (bid & 3) : ((bid >> 3) & 3); }
DI void handoff_sample(unsigned long long& smp, char* ws, int bid, int lane) {
  gu64* p = flag_word(ws, peer_of(bid, lane), (lane >> 2) & 1);
  asm volatile("global_load_dwordx2 %0, %1, off sc1" : "+v"(smp) : "v"(p) : "memory");
}
DI void handoff_answer(char* ws, int bid, int lane, int kind, unsigned long long smp) {
  const int peer = peer_of(bid, lane);
  if ((lane >> 2) == kind && peer != bid) st_agent(flag_word(ws, peer, 2 + 4 * kind + me_of(bid, kind)), smp ^ ACK_KEY);
}
DI void acks_fetch(unsigned long long& ack, char* ws, int bid, int lane) {
  gu64* p = flag_word(ws, bid, 2 + (lane & 7));
  asm volatile("global_load_dwordx2 %0, %1, off sc1" : "+v"(ack) : "v"(p) : "memory");
}
DI void handoff_raise(char* ws, int bid, int lane, int kind, unsigned long long smp, unsigned long long ack, unsigned step) {
  const int me = me_of(bid, kind);
  const unsigned long long own = __shfl(smp, 4 * kind + me);
  const bool chk = (lane >> 2) == kind && (lane & 3) != me;
  if (!__all(!chk || ack == (own ^ ACK_KEY))) {
    for (unsigned spins = 0; spins < SPIN_CAP; ++spins) {
      const bool ok = !chk || ld_agent(flag_word(ws, bid, 2 + (lane & 7))) == (own ^ ACK_KEY);
      if (__all(ok)) break;
      __builtin_amdgcn_s_sleep(1);
    }
  }
  if (lane == 0) st_agent(flag_word(ws, bid, kind), own + step);
}
DI void handoff_wait(char* ws, int bid, int lane, int kind, unsigned long long smp, unsigned step) {
  const int peer = peer_of(bid, lane);
  const bool chk = ((lane >> 2) & 15) == kind && peer != bid;
  gu64* p = flag_word(ws, peer, (lane >> 2) & 1);
  const unsigned base = (unsigned)smp + step, lim = chk ? 2u - step : 0xFFFFFFFFu;
  unsigned t, n = SPIN_CAP;
  asm volatile("1:\n\tglobal_load_dword %0, %2, off sc1\n\ts_waitcnt vmcnt(0)\n\tv_sub_u32 %0, %0, %3\n\tv_cmp_gt_u32 vcc, %0, %4\n\t"
               "s_cbranch_vccz 2f\n\ts_sleep 1\n\ts_sub_u32 %1, %1, 1\n\ts_cbranch_scc0 1b\n2:"
               : "=&v"(t), "+s"(n) : "v"(p), "v"(base), "v"(lim) : "vcc", "scc", "memory");
}

#define VMASK 0x35FFu
#define KMASK 0x0AFFu
constexpr int nth_bit(unsigned m, int n) { for (int t = 0; t < 16; ++t) if ((m >> t) & 1u) { if (n == 0) return t; --n; } return -1; }
constexpr int ops_of(int t) { return t <= 12 ? 8 : t == 13 ? 15 + (((VMASK >> 13) & 1u) ? 4 : 0) : t == 14 ? 5 : 0; }
constexpr int ops_between(int a, int b) { int n = 0; for (int s = a; s < b; ++s) n += ops_of(s); return n; }
constexpr int vm_top(int t) { int n = 63;
  if (t >= 4) { const int iv = nth_bit(VMASK, t - 4); const int y = (iv <= 12 ? 4 : 15) + ops_between(iv + 1, t); n = y < n ? y : n; }
  if (t + 2 >= 6 && t + 2 <= 15) { const int ik = nth_bit(KMASK, t - 4); const int y = ops_between(ik + 1, t); n = y < n ? y : n; }
  if (t == 0) { const int y = NP == 0 ? 4 * 6 : 4 * (7 - NP); n = y < n ? y : n; }
  if (t <= 3 && 2 * t + 3 >= 3 + NP) { const int y = 4 * (6 - 2 * t) + ops_between(0, t); n = y < n ? y : n; }
  return n; }
constexpr int vm_loop() { int n = 63; for (int t = 1; t <= 12; ++t) n = vm_top(t) < n ? vm_top(t) : n; return n; }
static_assert(__builtin_popcount(VMASK) == 12 && __builtin_popcount(KMASK) == 10, "twelve V tiles and ten K tiles follow the first ten units");
constexpr int park_at(int step) { constexpr int ps[6] = PSTEPS; for (int i = 0; i < NP; ++i) if (ps[i] == step) return i; return -1; }
struct KV { srd_t k, v; unsigned hoff, rot; unsigned voffK[4], voffV[2]; };
DI unsigned kv_soff(const KV& kv, int wv, int tile, bool valid) { return valid ? kv.hoff + (unsigned)(wv >> 1) * HEAD_BYTES + (((unsigned)tile + kv.rot) & 15u) * 8192u + (unsigned)(wv & 1) * 4096u : OOB; }
template <int P> DI void pieceK(const KV& kv, unsigned so) { dmal<P * 1024u, true>(kv.voffK[P], kv.k, so); }
template <int P> DI void pieceV(const KV& kv, unsigned so) { dmal<P * 1024u, true>(kv.voffV[P & 1], kv.v, so); }
template <bool ISV> DI void issueKV(const KV& kv, unsigned ldsb, int wv, int tile, unsigned slot) {
  unsigned so = kv_soff(kv, wv, tile, true), d = ldsb + slot * UNIT + (unsigned)wv * 4096u;
  setm0<0>(d); snop0();
  sfor<4>([&](auto I) INL { if constexpr (ISV) pieceV<I>(kv, so); else pieceK<I>(kv, so); });
}
constexpr int rank_of(int m, std::initializer_list<int> l) { int r = 0; for (int v : l) { if (v == m) return r; ++r; } return -1; }

DI void qphase(RSP unsigned ldsb, int w, int lane, srd_t srdW, srd_t srdX, const KV& kv, unsigned wcol, unsigned xrow0, int rot, char* ws, int bid, unsigned long long& smp, srd_t srdO, unsigned otouch, srd_t srdOut, unsigned outtouch) {
  unsigned junk = 0;
  const int hh = w >> 1, rh = w & 1;
  const int g = lane >> 4, q = (lane >> 2) & 3, p = lane & 3, r16 = lane & 15;
  constexpr unsigned WOFF[4] = {0u, 1024u, 2056u, 3080u}, WSTEP = 32u * 8192u, WWRAP = 18u * WSTEP;
  unsigned voW[4], voX[5];
  sfor<4>([&](auto I) INL { constexpr int i = I; voW[i] = (lane >> 4) * 8192u + (((lane & 15) ^ ((lane >> 4) << 2)) * 16u) + i * 32768u - WOFF[i]; });
  sfor<5>([&](auto I) INL { constexpr int n = I; voX[n] = (lane >> 3) * 1152u + (((lane & 7) ^ ((4 * ((w + n) & 1) + (lane >> 4)) & 7)) * 16u) + n * 9216u - (n < 3 ? n : n - 3) * 1024u; });
  const unsigned wsrc = wcol + (unsigned)hh * 256u + (unsigned)rh * 131072u, wdst = ldsb + WQB + (unsigned)hh * WQ_HEAD + (unsigned)rh * 4112u;
  const unsigned xsrc = xrow0 * 1152u + (unsigned)w * 5u * 9216u, xdst = ldsb + XB + (unsigned)w * 5120u;
  unsigned runW = (unsigned)(2 * rot) * WSTEP, runX = (unsigned)rot * 128u;
  auto nextW = [&]() INL { runW += WSTEP; runW = runW == WWRAP ? 0u : runW; };
  auto nextX = [&]() INL { runX += 128u; runX = runX == 9u * 128u ? 0u : runX; };
  auto loadW = [&](auto BUF) INL { unsigned so = wsrc + runW; pin(so); setm0<BUF * WQ_BUF>(wdst); snop0(); sfor<4>([&](auto I) INL { dmal<WOFF[I], WQ_NT>(voW[I], srdW, so); }); nextW(); };
  auto loadXa = [&](auto BUF) INL { unsigned so = xsrc + runX; pin(so); setm0<BUF * X_BUF>(xdst); snop0(); sfor<3>([&](auto I) INL { dmal<I * 1024u, X_NT>(voX[I], srdX, so); }); };
  auto loadXb = [&](auto BUF) INL { unsigned so = xsrc + runX; pin(so); setm0<BUF * X_BUF + 3072u>(xdst); snop0(); sfor<2>([&](auto I) INL { dmal<I * 1024u, X_NT>(voX[3 + I], srdX, so); }); nextX(); };
  RST(15);
  loadW(IC<0>{}); loadXa(IC<0>{}); loadXb(IC<0>{});
  RST(22);
  asm volatile("s_barrier" ::: "memory");
  loadW(IC<1>{});
  {
    touch(junk, 0u, srdO, otouch); touch(junk, 0u, srdOut, outtouch);
  }
  unsigned wb[4], xb[2];
#pragma unroll
  for (int s = 0; s < 4; ++s) { wb[s] = ldsb + WQB + hh * WQ_HEAD + g * 2056u + q * 256u + ((s ^ q) * 64u) + p * 16u; pinv(wb[s]); }
#pragma unroll
  for (int k2 = 0; k2 < 2; ++k2) { xb[k2] = ldsb + XB + rh * 10240u + r16 * 128u + (((4 * k2 + g) ^ (r16 >> 1)) * 16u); pinv(xb[k2]); }
  RST(23);
  wait_bar<6>();
  RST(1);
  loadW(IC<2>{}); loadXa(IC<1>{}); loadXb(IC<1>{}); loadXa(IC<2>{});
  asm volatile("" : "+v"(smp));
  if (w == 3) { handoff_answer(ws, bid, lane, 0, smp); handoff_answer(ws, bid, lane, 1, smp); }
  bf16x4 alo[2][8], ahi[2][8]; bf16x8 bfr[2][5];
  sfor<8>([&](auto T) INL { constexpr int t = T; alo[0][t] = ldstr1(wb[t >> 1] + (t & 1) * 8u); ahi[0][t] = ldstr1(wb[t >> 1] + (t & 1) * 8u + 1024u); });
  sfor<5>([&](auto T) INL { bfr[0][T] = lds128(xb[0] + T * 2048u); });
  unsigned soW = wsrc + runW, soX = xsrc + runX;
  int leftW = 15, leftX = 6;
  sfor<3>([&](auto IT) INL {
    constexpr int it = IT;
    sfor<6>([&](auto J) INL {
      constexpr int j = J, k2 = j & 1, cur = j & 1, nxt = cur ^ 1;
      constexpr int wnb = (j + 1) % 3;
      constexpr int xnb = ((j + 1) >> 1) % 3;
      constexpr int xbuf = k2 == 1 ? (j >> 1) % 3 : ((j >> 1) + 2) % 3;
      constexpr int pos = 6 * it + j;
      constexpr auto extra = [](int s) { return (s >= 14 && s <= 16) || park_at(s) >= 0 ? 4 : 0; };
      constexpr auto xpart = [](int s) { return s >= 13 ? 0 : (s & 1) ? 3 : 2; };
      constexpr auto wpart = [](int s) { return s >= 15 ? 0 : 4; };
      wait_bar<pos == 0 ? 14 : pos == 1 ? 3 + 4 + extra(0) + 2 : pos == 17 ? 63 : extra(pos - 2) + xpart(pos - 2) + wpart(pos - 1) + extra(pos - 1) + xpart(pos - 1)>();
      const unsigned sw = soW, sx = soX; unsigned skv = 0;
      sfor<40>([&](auto M) INL {
        constexpr int m = M, t = m / 5, qt = m % 5;
        if constexpr (pos == 0) mfa0<AO(t, qt)>(cat(alo[cur][t], ahi[cur][t]), bfr[cur][qt]); else mfa<AO(t, qt)>(cat(alo[cur][t], ahi[cur][t]), bfr[cur][qt]);
        GAP();
        constexpr int ri = rank_of(m, {0, 2, 3, 4, 6, 7, 8, 10, 11, 12, 14, 15, 16, 18, 19, 20, 22, 23, 24, 26, 27});
        if constexpr (ri >= 0 && ri < 5) bfr[nxt][ri] = lds128(xb[k2 ^ 1] + xnb * X_BUF + ri * 2048u);
        if constexpr (ri >= 5) { constexpr int i = ri - 5, t2 = i >> 1;
          if constexpr ((i & 1) == 0) alo[nxt][t2] = ldstr1(wb[t2 >> 1] + wnb * WQ_BUF + (t2 & 1) * 8u); else ahi[nxt][t2] = ldstr1(wb[t2 >> 1] + wnb * WQ_BUF + (t2 & 1) * 8u + 1024u); }
        if constexpr (m == 0) setm0<(j % 3) * WQ_BUF>(wdst);
        if constexpr (pos < 15 && (m == 1 || m == 5 || m == 9 || m == 13)) dmal<WOFF[(m - 1) / 4], WQ_NT>(voW[(m - 1) / 4], srdW, sw);
        if constexpr (m == 32) setm0<xbuf * X_BUF + (k2 == 1 ? 0u : 3072u)>(xdst);
        if constexpr (pos < 13 && (m == 33 || m == 36 || (m == 39 && k2 == 1))) dmal<((m - 33) / 3) * 1024u, X_NT>(voX[(k2 == 1 ? 0 : 3) + (m - 33) / 3], srdX, sx);
        if constexpr (park_at(pos) >= 0) { constexpr int i = park_at(pos);
          if constexpr (m == 15) { skv = kv_soff(kv, w, (i & 1) ? 1 + i / 2 : 2 + i / 2, true); pin(skv); }
          if constexpr (m == 17 || m == 21 || m == 25 || m == 29) { constexpr int pc = (m - 17) / 4;
            if constexpr (i & 1) loada<160 + 16 * i + 4 * pc, pc * 1024u>(kv.voffV[pc & 1], kv.v, skv); else loada<160 + 16 * i + 4 * pc, pc * 1024u>(kv.voffK[pc], kv.k, skv); }
        }
        if constexpr (pos >= 14 && pos <= 16) {
          if constexpr (m == 15) { skv = kv_soff(kv, w, pos == 15 ? 1 : 0, true); pin(skv); }
          if constexpr (m == 16) setm0<(pos - 14) * UNIT>(ldsb + (unsigned)w * 4096u);
          if constexpr (m == 17 || m == 21 || m == 25 || m == 29) { if constexpr (pos == 16) pieceV<(m - 17) / 4>(kv, skv); else pieceK<(m - 17) / 4>(kv, skv); }
        }
        if constexpr (m == 28) { runW += WSTEP; pin(runW); }
        if constexpr (m == 30) { runW = runW == WWRAP ? 0u : runW; pin(runW); }
        if constexpr (m == 31) { leftW -= 1; soW = (wsrc + runW) | ((unsigned)((leftW - 1) >> 31) & OOB); pin(soW); }
        if constexpr (k2 == 0) {
          if constexpr (m == 37) { runX += 128u; pin(runX); }
          if constexpr (m == 38) { runX = runX == 9u * 128u ? 0u : runX; pin(runX); }
          if constexpr (m == 39) { leftX -= 1; soX = (xsrc + runX) | ((unsigned)(leftX >> 31) & OOB); pin(soX); }
        }
        GAP();
      });
    });
  });
  wait_bar<8>();
  RST(4);
  asm volatile("" :: "v"(junk));
  sfor<7>([&](auto I) INL { constexpr int i = I; if constexpr (i >= NP) { if constexpr (i & 1) issueKV<true>(kv, ldsb, w, 1 + i / 2, 3 + i); else issueKV<false>(kv, ldsb, w, 2 + i / 2, 3 + i); } });
  GAP();
}

constexpr unsigned OBUF[4] = {0u, 20480u, 86016u, 124928u}, WBUF[4] = {49152u, 67584u, 106496u, 145408u};
struct OP { srd_t o, wo; unsigned obase, wobase, voffO, voffWo[5], wpi0; bool five; };
template <int N> DI void wo_load(const OP& op, unsigned so) { dmal<(N & 3) * 1024u, WO_NT>(op.voffWo[N], op.wo, so); }
template <int BUF> DI void wo_issue(const OP& op, unsigned ldsb, unsigned so) {
  setm0<WBUF[BUF]>(ldsb + op.wpi0 * 1024u); snop0();
  sfor<4>([&](auto N) INL { wo_load<N>(op, so); });
  if (op.five) { setm0<WBUF[BUF] + 4096u>(ldsb + op.wpi0 * 1024u); snop0(); wo_load<4>(op, so); }
}
template <int BUF> DI void o_issue(const OP& op, unsigned ldsb, int w, unsigned so) {
  setm0<OBUF[BUF]>(ldsb + (unsigned)w * 5120u); snop0();
  sfor<4>([&](auto N) INL { dmal_sc1<N * 1024u>(op.voffO, op.o, so); });
  setm0<OBUF[BUF] + 4096u>(ldsb + (unsigned)w * 5120u); unsigned s2 = so + 4096u; pin(s2);
  dmal_sc1<0>(op.voffO, op.o, s2);
}
DI unsigned stage_of(int pos, int hp, int rb) {
  const int i = pos - 4, half = (i + 2) >> 3; int r = i - 6 * half + rb; r -= 6 & ((5 - r) >> 31);
  return 4 * ((hp + 1 + (r >> 1)) & 3) + 2 * (r & 1) + (half & 1); }

template <int NHF>
DI void oproj(RSP unsigned ldsb, int w, int lane, int bid, const OP& op, char* ws, unsigned long long smp, unsigned long long ack, char* outp,
              const u32x4 (&late)[10], unsigned lateoff) {
  const int rh = w >> 1, g = lane >> 4, q = (lane >> 2) & 3, p = lane & 3, hp = (bid >> 3) & 3, rb = OROT;
  auto soO = [&](int pos) INL { return pos < 16 ? op.obase + stage_of(pos, hp, rb) * 20480u + (unsigned)w * 5120u : OOB; };
  auto soWo = [&](int pos) INL { return pos < 16 ? op.wobase + stage_of(pos, hp, rb) * (64u * 1152u) : OOB; };
  const unsigned ab = ldsb + (4 * g + q) * 288u + p * 8u, bb = ldsb + rh * 5120u + lane * 16u;
  unsigned abuf[4], bbuf[4];
#pragma unroll
  for (int i = 0; i < 4; ++i) { abuf[i] = ab + WBUF[i]; bbuf[i] = bb + OBUF[i]; pinv(abuf[i]); pinv(bbuf[i]); }
  const unsigned wdst = ldsb + op.wpi0 * 1024u, w5mul = op.five ? 1u : 0u, w5base = op.five ? wdst + 4096u : ldsb + 40960u, w5oob = op.five ? 0u : OOB;
  bf16x4 alo[2][5], ahi[2][5]; bf16x8 bfr[2][5];
  auto ntof = [](int a) { return a == 4 ? 4 : NHF * 5 + a; };
  sfor<5>([&](auto A) INL { constexpr int a = A; if constexpr (a < 4 || NHF == 0) { alo[0][a] = ldstr1(abuf[0] + ntof(a) * 32u); ahi[0][a] = ldstr1(abuf[0] + ntof(a) * 32u + 4608u); } });
  sfor<5>([&](auto T) INL { bfr[0][T] = lds128(bbuf[0] + T * 1024u); });
  unsigned soN = 0, swN = 0, sw3 = 0;
  auto kstep = [&](auto K2_, auto RB_, auto DMA_, auto DB_, auto FIRST_, unsigned so, auto&& tail) INL {
    constexpr int k2 = K2_, rb = RB_, dm = DMA_, db = DB_; constexpr bool first = FIRST_;
    constexpr int na = (k2 == NHF) ? 5 : 4, nan = ((k2 ^ 1) == NHF) ? 5 : 4;
    constexpr unsigned aoff = (k2 == 0 ? 9216u : 0u), boff = (k2 == 0 ? 10240u : 0u);
    unsigned s2 = so;
    sfor<na * 5>([&](auto M) INL {
      constexpr int m = M, a = m / 5, qt = m % 5;
      if constexpr (first && (k2 == 0 || a == 4)) mfa0<AO(a, qt)>(cat(alo[k2][a], ahi[k2][a]), bfr[k2][qt]);
      else mfa<AO(a, qt)>(cat(alo[k2][a], ahi[k2][a]), bfr[k2][qt]);
      GAP();
      constexpr int ri = rank_of(m, {0, 2, 3, 4, 6, 7, 8, 10, 11, 12, 14, 15, 16});
      auto rd = [&](auto I_) INL { constexpr int i = I_;
        if constexpr (i < 5) bfr[k2 ^ 1][i] = lds128(bbuf[rb] + boff + i * 1024u);
        else if constexpr (i - 5 < 2 * nan) { constexpr int a2 = (i - 5) >> 1;
          if constexpr (((i - 5) & 1) == 0) alo[k2 ^ 1][a2] = ldstr1(abuf[rb] + aoff + ntof(a2) * 32u); else ahi[k2 ^ 1][a2] = ldstr1(abuf[rb] + aoff + ntof(a2) * 32u + 4608u); } };
      if constexpr (ri >= 0) rd(IC<ri < 0 ? 0 : ri>{});
      if constexpr (m == 15) rd(IC<13>{});
      if constexpr (m == 16) rd(IC<14>{});
      if constexpr (dm == 1) {
        if constexpr (m == 0) setm0<OBUF[db]>(ldsb + (unsigned)w * 5120u);
        if constexpr (m == 1 || m == 5 || m == 9 || m == 13) dmal_sc1<((m - 1) / 4) * 1024u>(op.voffO, op.o, s2);
        if constexpr (m == 14) { setm0<OBUF[db] + 4096u>(ldsb + (unsigned)w * 5120u); s2 = so + 4096u; pin(s2); }
        if constexpr (m == 17) dmal_sc1<0>(op.voffO, op.o, s2);
      }
      if constexpr (dm == 2) {
        if constexpr (m == 0) setm0<WBUF[db]>(wdst);
        if constexpr (m == 1 || m == 5 || m == 9 || m == 13) wo_load<(m - 1) / 4>(op, s2);
        if constexpr (m == 14) { setm0<0>(w5base + w5mul * WBUF[db]); s2 = so | w5oob; pin(s2); }
        if constexpr (m == 17) wo_load<4>(op, s2);
      }
      if constexpr (dm == 3) {
        if constexpr (m < 20 && m % 4 == 0) { constexpr int n = m / 4; if constexpr (n < 4) { setm0<OBUF[db] + n * 1024u>(ldsb + (unsigned)w * 5120u); s2 = so + n * 1024u; } else { setm0<OBUF[db] + 4096u>(ldsb + (unsigned)w * 5120u); s2 = so + 4096u; } pin(s2); }
        if constexpr (m < 20 && m % 4 == 1) dmal_sc1<0>(op.voffO, op.o, s2);
        if constexpr (m < 20 && m % 4 == 2) { constexpr int n = m / 4; if constexpr (n < 4) { setm0<WBUF[db]>(wdst); s2 = sw3; } else { setm0<0>(w5base + w5mul * WBUF[db]); s2 = sw3 | w5oob; } pin(s2); }
        if constexpr (m < 20 && m % 4 == 3) wo_load<m / 4>(op, s2);
      }
      tail(M);
      GAP();
    });
  };
  auto none = [](auto) {};
  using F_ = std::false_type; using T_ = std::true_type;
  auto late_store = [&](auto K) { return [&](auto M) INL { constexpr int m = M; if constexpr (m % 4 == (K == 0 ? 3 : 1) && m / 4 < 5) { constexpr int i = 5 * K + m / 4;
    __builtin_amdgcn_raw_buffer_store_b128(late[i], op.o, lane * 16u + ((2 + i / 5) * 10 + i % 5) * 1024u, lateoff, 16); } }; };
  kstep(IC<0>{}, IC<0>{}, IC<0>{}, IC<0>{}, T_{}, 0u, none); kstep(IC<1>{}, IC<1>{}, IC<0>{}, IC<0>{}, T_{}, 0u, none);
  RST(16);
  bar_only();
  kstep(IC<0>{}, IC<1>{}, IC<2>{}, IC<0>{}, F_{}, soWo(4), late_store(IC<0>{})); kstep(IC<1>{}, IC<2>{}, IC<0>{}, IC<0>{}, F_{}, 0u, late_store(IC<1>{}));
  RST(17);
  wait_bar<15>();
  if (w == 3) handoff_raise(ws, bid, lane, 0, smp, ack, 1);
  RST(10);
  kstep(IC<0>{}, IC<2>{}, IC<2>{}, IC<1>{}, F_{}, soWo(5), none); kstep(IC<1>{}, IC<3>{}, IC<0>{}, IC<0>{}, F_{}, 0u, none);
  RST(18);
  if (w == 3) handoff_wait(ws, bid, lane, 0, smp, 1);
  bar_only();
  RST(11);
  o_issue<0>(op, ldsb, w, soO(4));
  kstep(IC<0>{}, IC<3>{}, IC<1>{}, IC<1>{}, F_{}, soO(5), none);
  RST(19);
  wait_bar<5>();
  RST(20);
  if (w == 3) handoff_raise(ws, bid, lane, 0, smp, ack, 2);
  soN = soO(7); swN = soWo(7);
  sw3 = soWo(6);
  kstep(IC<1>{}, IC<0>{}, IC<3>{}, IC<2>{}, F_{}, soO(6), none);
  RST(12);
  for (int it = 1; it < 4; ++it) {
    sfor<4>([&](auto J) INL {
      constexpr int jb = J, nb = (jb + 1) & 3, db = (jb + 3) & 3;
      const int pos = 4 * it + jb;
      if constexpr (jb == 3) { if (w == 3 && it == 1) handoff_wait(ws, bid, lane, 0, smp, 2); }
      wait_bar<10>();
      const unsigned so = soN, sw = swN;
      kstep(IC<0>{}, IC<jb>{}, IC<1>{}, IC<db>{}, F_{}, so, none);
      unsigned st = 0, msk = 0;
      kstep(IC<1>{}, IC<nb>{}, IC<2>{}, IC<db>{}, F_{}, sw, [&](auto M) INL {
        if constexpr (M == 3) { st = stage_of(pos + 4, hp, rb); pin(st); }
        if constexpr (M == 7) { msk = (unsigned)((11 - pos) >> 31) & OOB; pin(msk); }
        if constexpr (M == 11) { soN = (op.obase + st * 20480u + (unsigned)w * 5120u) | msk; pin(soN); }
        if constexpr (M == 15) { swN = (op.wobase + st * (64u * 1152u)) | msk; pin(swN); } });
    });
  }
  wait_bar<0>();
  RST(13);
  asm volatile("s_nop 7" ::: "memory");
  if constexpr (NHF == 1) sfor<5>([&](auto QT) INL { *(AS3 f32x4*)(size_t)(ldsb + (rh * 5 + QT) * 1024u + lane * 16u) = aread<AO(4, QT)>(); });
  bar_only();
  u32x2 shp[5]; f32x4 sh[5];
  sfor<5>([&](auto QT) INL { constexpr int qt = QT;
    if constexpr (NHF == 0) { sh[qt] = aread<AO(4, qt)>() + *(const AS3 f32x4*)(size_t)(ldsb + (rh * 5 + qt) * 1024u + lane * 16u); shp[qt] = u32x2{cvtpk(sh[qt][0], sh[qt][1]), cvtpk(sh[qt][2], sh[qt][3])}; }
    else shp[qt] = u32x2{0u, 0u}; });
  const int hg = bid & 3;
  const srd_t sp = mksrd(ws + WS_PART, (unsigned)PART_BYTES);
  auto inbox = [&](int owner_hg, int src_hg) INL { return (unsigned)((((bid & ~3) | owner_hg) * 4 + src_hg) * 4 + w) * 4096u; };
  auto pk = [](f32x4 v) { return u32x2{cvtpk(v[0], v[1]), cvtpk(v[2], v[3])}; };
  sfor<4>([&](auto A) INL { constexpr int a = A;
    if (a != hg) { const unsigned base = inbox(a, hg);
      const u32x2 t0 = pk(aread<AO(a, 0)>()), t1 = pk(aread<AO(a, 1)>()), t2 = pk(aread<AO(a, 2)>()), t3 = pk(aread<AO(a, 3)>()), t4 = pk(aread<AO(a, 4)>());
      const int qs = (a - rh) & 3;
      const u32x2 s = qs == 0 ? shp[0] : qs == 1 ? shp[1] : qs == 2 ? shp[2] : shp[3];
      __builtin_amdgcn_raw_buffer_store_b128(u32x4{t0[0], t0[1], t1[0], t1[1]}, sp, lane * 16u, base, 16);
      __builtin_amdgcn_raw_buffer_store_b128(u32x4{t2[0], t2[1], t3[0], t3[1]}, sp, lane * 16u + 1024u, base, 16);
      __builtin_amdgcn_raw_buffer_store_b128(u32x4{t4[0], t4[1], s[0], s[1]}, sp, lane * 16u + 2048u, base, 16);
      if (NHF == 0 && a == rh) __builtin_amdgcn_raw_buffer_store_b128(u32x4{shp[4][0], shp[4][1], 0u, 0u}, sp, lane * 16u + 3072u, base, 16);
    } });
  const int b = bid >> 5, nq = (bid >> 3) & 3, br = (bid & 7) >> 2;
  char* orow = outp + ((size_t)(b * SQ + rh * 80 + (lane & 15)) * 1152 + br * 576 + nq * 144 + g * 4) * 2;
  auto up = [](unsigned lo, unsigned hi) { return f32x4{__uint_as_float(lo << 16), __uint_as_float(lo & 0xFFFF0000u), __uint_as_float(hi << 16), __uint_as_float(hi & 0xFFFF0000u)}; };
  wait_bar<0>();
  if (w == 3) { handoff_raise(ws, bid, lane, 1, smp, ack, 1); handoff_wait(ws, bid, lane, 1, smp, 1); }
  bar_only();
  RST(14);
  u32x4 in[3][4];
  sfor<3>([&](auto S) INL { const unsigned base = inbox(hg, (hg + 1 + S) & 3);
    sfor<3>([&](auto G) INL { in[S][G] = __builtin_amdgcn_raw_buffer_load_b128(sp, lane * 16u + G * 1024u, base, 16); });
    in[S][3] = __builtin_amdgcn_raw_buffer_load_b128(sp, lane * 16u + 3072u, (NHF == 0 && hg == rh) ? base : OOB, 16); });
  sfor<4>([&](auto A) INL { constexpr int a = A;
    if (a == hg) sfor<5>([&](auto QT) INL { constexpr int qt = QT;
      f32x4 s = aread<AO(a, qt)>();
      sfor<3>([&](auto S) INL { s += up(in[S][qt >> 1][2 * (qt & 1)], in[S][qt >> 1][2 * (qt & 1) + 1]); });
      *(u32x2*)(orow + (size_t)qt * 16 * 2304 + ntof(a) * 32) = pk(s); }); });
  if constexpr (NHF == 0) {
    const int qs = (hg - rh) & 3;
    f32x4 s = qs == 0 ? sh[0] : qs == 1 ? sh[1] : qs == 2 ? sh[2] : sh[3];
    sfor<3>([&](auto S) INL { s += up(in[S][2][2], in[S][2][3]); });
    *(u32x2*)(orow + (size_t)qs * 16 * 2304 + 4 * 32) = pk(s);
    if (hg == rh) { f32x4 s4 = sh[4];
      sfor<3>([&](auto S) INL { s4 += up(in[S][3][0], in[S][3][1]); });
      *(u32x2*)(orow + (size_t)4 * 16 * 2304 + 4 * 32) = pk(s4); }
  }
}

__global__ __launch_bounds__(256, 1) void fused_kernel(const char* xA, const char* kA, const char* vA, const char* wqA, const char* woA,
                                                       const char* xB_, const char* kB, const char* vB, const char* wqB, const char* woB,
                                                       char* outp, char* ws) {
  extern __shared__ __attribute__((aligned(16))) char lds[];
  const unsigned ldsb = (unsigned)(size_t)(AS3 char*)lds;
  const int tid = threadIdx.x, lane = tid & 63, w = __builtin_amdgcn_readfirstlane(tid >> 6);
  const int bid = blockIdx.x, j = bid & 7, br = j >> 2, hg = j & 3, b = bid >> 5, hp = (bid >> 3) & 3;
  const int h0 = hg * 8 + hp * 2, hh = w >> 1, rh = w & 1;
  const int g = lane >> 4, q = (lane >> 2) & 3, p = lane & 3, r16 = lane & 15;
  srd_t srdX = mksrd(br ? xB_ : xA, X_BYTES), srdW = mksrd(br ? wqB : wqA, W_BYTES);
  KV kv; kv.k = mksrd(br ? kB : kA, KV_BYTES); kv.v = mksrd(br ? vB : vA, KV_BYTES);
  kv.hoff = (unsigned)(b * NH + h0) * HEAD_BYTES; kv.rot = KVROT;
#pragma unroll
  for (int pc = 0; pc < 4; ++pc) kv.voffK[pc] = (lane >> 4) * 256u + (((lane & 15) ^ (4 * pc + (lane >> 4))) * 16u);
#pragma unroll
  for (int pc = 0; pc < 2; ++pc) kv.voffV[pc] = (lane >> 4) * 256u + (((lane & 15) ^ (((4 * pc + (lane >> 4)) & 7) << 1)) * 16u);
  unsigned long long smp = 0, ack = 0;
  if (w == 3) handoff_sample(smp, ws, bid, lane);

  own_acc();
  unsigned junkc = 0;
  if (w == 2) { unsigned long long pc; unsigned len;
    asm volatile("s_getpc_b64 %0\n.Lcode_from%=:\n\ts_mov_b32 %1, fused_code_end-.Lcode_from%=" : "=s"(pc), "=s"(len));
    const srd_t srdC = mksrd((const void*)pc, len);
    touch(junkc, lane < 24 ? ((unsigned)(bid >> 3) * 24u + lane) * 128u : OOB, srdC, 0u); }
  qphase(RSA ldsb, w, lane, srdW, srdX, kv, (unsigned)h0 * 256u, (unsigned)b * SQ, ROT, ws, bid, smp, mksrd(ws + WS_OF, (unsigned)OF_BYTES), (unsigned)(((br * 8 + b) * 32 + h0 + hh) * 40) * 1024u,
         mksrd(outp, 2949120u), (unsigned)(b * SQ + w * 40) * 2304u);
  asm volatile("" :: "v"(junkc));
  unsigned ka[4], va[8];
#pragma unroll
  for (int s = 0; s < 4; ++s) ka[s] = ldsb + hh * 8192u + r16 * 256u + (((4 * s + g) ^ r16) * 16u);
#pragma unroll
  for (int dt = 0; dt < 8; ++dt) va[dt] = ldsb + 2 * UNIT + hh * 8192u + (4 * g + q) * 256u + ((dt ^ ((4 * g + q) & 7)) * 32u) + p * 8u;
  f32x4 L3 = {0, 0, 0, 0}, L4 = {0, 0, 0, 0}, negm[5], S0[2][5], S1[2][5];
  bf16x8 kf[2][4];
  sfor<8>([&](auto I) INL { kf[I / 4][I % 4] = lds128(ka[I % 4] + (I / 4) * 4096u); });
  const unsigned dpa = ldsb + 3 * UNIT + (unsigned)w * 4096u + lane * 16u, dpb = dpa + 3 * UNIT;
  auto dump = [&](auto C_) INL { constexpr int c = C_; if constexpr (c < NP) sfor<4>([&](auto I) INL { dswa<160 + 16 * c + 4 * I, (c % 3) * UNIT + I * 1024u>(c < 3 ? dpa : dpb); }); };
  dump(IC<0>{}); dump(IC<1>{});
  GAP();
  asm volatile("s_nop 3" ::: "memory");
  sfor<25>([&](auto I) INL { constexpr int i = I, qt = i / 4, s = i % 4;
    if constexpr (s == 0 && i <= 20) { __builtin_amdgcn_s_waitcnt(i < 20 ? 0xC47F : 0xC07F); if constexpr (qt + 2 < 6) dump(IC<qt + 2 < 6 ? qt + 2 : 0>{}); GAP(); }
    if constexpr (i < 20) awrite<AQ(qt, s)>(__builtin_bit_cast(u32x4, pack8(aread<AO(2 * s, qt)>() * QSCALE, aread<AO(2 * s + 1, qt)>() * QSCALE)));
    if constexpr (i >= 4 && i < 24) { constexpr int pq = qt - 1;
      GAP();
      sfor<2>([&](auto E) INL { constexpr int e = 2 * s + E, kt = e >> 2, s2 = e & 3;
        if constexpr (s2 == 0) mfsz<AQ(pq, 0)>(S0[kt][pq], kf[kt][0]); else mfs<AQ(pq, s2)>(S0[kt][pq], kf[kt][s2]); });
      GAP(); }
    if constexpr (i == 20) { awrite<AONES>(u32x4{0x3F803F80u, 0x3F803F80u, 0x3F803F80u, 0x3F803F80u}); azero<AL0>(); azero<AL0 + 4>(); azero<AL0 + 8>(); } });
  GAP();
  wait_bar<4 + 4 * (7 - NP)>();
  sfor<8>([&](auto I) INL { kf[I / 4][I % 4] = lds128(ka[I % 4] + UNIT + (I / 4) * 4096u); });
  sfor<4>([&](auto I) INL { ka[I] += 3 * UNIT; });
  nop16();
  sfor<5>([&](auto QT) INL { constexpr int qt = QT;
    float m = max3f(max3f(S0[0][qt][0], S0[0][qt][1], S0[0][qt][2]), max3f(S0[0][qt][3], S0[1][qt][0], S0[1][qt][1]), fmaxf(S0[1][qt][2], S0[1][qt][3]));
    m = xmax16_32(m);
    negm[qt] = f32x4{-m, -m, -m, -m};
    S0[0][qt] -= m; S0[1][qt] -= m; });
  GAP();
  RST(5);
  OP op; op.o = mksrd(ws + WS_OF, (unsigned)OF_BYTES); op.wo = mksrd(br ? woB : woA, W_BYTES);
  op.obase = (unsigned)((br * 8 + b) * 32 + hg * 8) * 40960u; op.wobase = (unsigned)hg * 1024u * 1152u + hp * 288u;
  op.wpi0 = (w < 2 ? 5 * w : 10 + 4 * (w - 2)); op.five = w < 2; op.voffO = lane * 16u;
  unsigned slotV = 2, adv = 2 * UNIT;
  unsigned dV = ldsb + (unsigned)w * 4096u, dK = dV + UNIT, soV = kv_soff(kv, w, 4, true), soK = kv_soff(kv, w, 6, true);
  const unsigned qb = kv.hoff + (unsigned)hh * HEAD_BYTES + (unsigned)rh * 4096u;
  unsigned tV = 4 + kv.rot, tK = 6 + kv.rot;
  unsigned cV = 0, cK = 1;
  unsigned fV = VMASK & 1u, fK = KMASK & 1u, sV = (VMASK & 1u) ? soV : OOB, sK = (KMASK & 1u) ? soK : OOB;
  const unsigned wdstA = ldsb + op.wpi0 * 1024u, w5mulA = op.five ? 1u : 0u, w5baseA = op.five ? wdstA + 4096u : ldsb + UNIT + (unsigned)w * 4096u, w5oobA = op.five ? 0u : OOB;
  auto iter = [&](int t, f32x4 (&Sc)[2][5], f32x4 (&Sn)[2][5], auto do_qk_, auto do_k_, auto first_, auto mode_, auto vm_) INL {
    constexpr bool do_qk = decltype(do_qk_)::value, do_k = decltype(do_k_)::value, first = decltype(first_)::value;
    constexpr int mode = mode_;
    wait_bar<vm_>();
    own_acc();
    u32x4 Pw[5]; float ex[40]; bf16x4 vlo[8], vhi[8];
    if constexpr (mode == 1) { if (w == 3) acks_fetch(ack, ws, bid, lane);
      if constexpr ((VMASK >> 13) & 1u) { setm0<0>(dV); snop0(); sfor<4>([&](auto I) INL { pieceV<I>(kv, sV); }); } }
    unsigned swo = 0;
    auto wo_set = [&](auto I_) INL { constexpr int i = I_, st = (mode == 2 ? 3 : 0) + i / 5, n = i % 5;
      if constexpr (n == 0) { swo = op.wobase + (unsigned)(4 * hp + st) * (64u * 1152u); pin(swo); setm0<WBUF[st]>(wdstA); }
      if constexpr (n == 4) { swo |= w5oobA; pin(swo); setm0<0>(w5baseA + w5mulA * WBUF[st]); } };
    auto wo_ld = [&](auto I_) INL { wo_load<I_ % 5>(op, swo); };
    GAP();
    sfor<40>([&](auto M) INL {
      constexpr int m = M, tile = m >> 2, s = m & 3, kt = tile / 5, qt = tile % 5;
      if constexpr (do_qk) { if constexpr (s == 0) mfs0<AQ(qt, 0)>(Sn[kt][qt], kf[kt][0], negm[qt]); else mfs<AQ(qt, s)>(Sn[kt][qt], kf[kt][s]); }
      GAP();
      constexpr int ga = m / 5, r = m % 5;
      if constexpr (r < 4) { constexpr int e = 4 * ga + r; ex[e] = __builtin_amdgcn_exp2f(Sc[(e & 7) >> 2][e >> 3][e & 3]); }
      else { constexpr int e = 4 * ga; Pw[e >> 3][(e & 7) >> 1] = cvtpk(ex[e], ex[e + 1]); Pw[e >> 3][((e & 7) >> 1) + 1] = cvtpk(ex[e + 2], ex[e + 3]); }
      if constexpr (m == 19) vlo[0] = ldstr1(va[0]);
      if constexpr (m == 24) vhi[0] = ldstr1(va[0] + 4096u);
      if constexpr (m == 29) vlo[1] = ldstr1(va[1]);
      if constexpr (m == 34) vhi[1] = ldstr1(va[1] + 4096u);
      if constexpr (mode == 0) {
        if constexpr (m == 2) setm0<0>(dV);
        if constexpr (m >= 3 && m < 7) pieceV<m - 3>(kv, sV);
      }
      if constexpr (mode == 1) { if constexpr (m % 5 == 1) wo_set(IC<m / 5>{}); if constexpr (m % 5 == 2) wo_ld(IC<m / 5>{}); }
      if constexpr (mode == 2) { if constexpr (m % 8 == 1) wo_set(IC<m / 8>{}); if constexpr (m % 8 == 2) wo_ld(IC<m / 8>{}); }
      GAP();
    });
    float mx0 = 0.f, mx1 = 0.f; unsigned long long bal = 0;
    const unsigned advc = adv;
    sfor<45>([&](auto M) INL {
      constexpr int m = M;
      constexpr int dt = m < 8 ? m / 4 : (m < 38 ? ((m - 8) % 5 < 4 ? 2 + (m - 8) / 5 : (m - 8) / 5) : m - 32), qt = m < 8 ? m % 4 : (m < 38 ? ((m - 8) % 5 < 4 ? (m - 8) % 5 : 4) : 4);
      if constexpr (m < 40) {
        if constexpr (first) mfa0<AO(dt, qt)>(cat(vlo[dt], vhi[dt]), __builtin_bit_cast(bf16x8, Pw[qt]));
        else mfa<AO(dt, qt)>(cat(vlo[dt], vhi[dt]), __builtin_bit_cast(bf16x8, Pw[qt])); }
      else if constexpr (m < 43) mfl<AL0 + 4 * (m - 40)>(__builtin_bit_cast(bf16x8, Pw[m - 40]));
      else if constexpr (m == 43) mflv(L3, __builtin_bit_cast(bf16x8, Pw[3]));
      else mflv(L4, __builtin_bit_cast(bf16x8, Pw[4]));
      GAP();
      if constexpr (m < 8) { constexpr int e = 32 + m; ex[e] = __builtin_amdgcn_exp2f(Sc[(e & 7) >> 2][4][e & 3]); }
      if constexpr (m == 8 || m == 9) { constexpr int e = 32 + 4 * (m - 8); Pw[4][2 * (m - 8)] = cvtpk(ex[e], ex[e + 1]); Pw[4][2 * (m - 8) + 1] = cvtpk(ex[e + 2], ex[e + 3]); }
      constexpr int vr = rank_of(m, {0, 1, 4, 5, 8, 9, 13, 14, 18, 19, 23, 24});
      if constexpr (vr >= 0) { constexpr int d2 = 2 + vr / 2;
        if constexpr ((vr & 1) == 0) vlo[d2] = ldstr1(va[d2]); else vhi[d2] = ldstr1(va[d2] + 4096u); }
      if constexpr (do_qk && m >= 10 && m < 20) { constexpr int tile = m - 10, kt = tile / 5, q2 = tile % 5;
        if constexpr (tile == 0) { mx0 = max3f(Sn[0][0][0], Sn[0][0][1], Sn[0][0][1]); mx1 = max3f(Sn[0][0][2], Sn[0][0][3], Sn[0][0][3]); }
        else { mx0 = max3f(mx0, Sn[kt][q2][0], Sn[kt][q2][1]); mx1 = max3f(mx1, Sn[kt][q2][2], Sn[kt][q2][3]); } }
      if constexpr (m >= 20 && m <= 22) { va[2 * (m - 20)] += advc; va[2 * (m - 20) + 1] += advc; pinv(va[2 * (m - 20)]); pinv(va[2 * (m - 20) + 1]); }
      if constexpr (m == 25) { va[6] += advc; va[7] += advc; pinv(va[6]); pinv(va[7]); }
      if constexpr (do_qk && m == 26) { bal = __ballot(!(max2f(mx0, mx1) <= THR)); asm volatile("" : "+s"(bal)); }
      if constexpr (do_k && m >= 26 && m < 34) { constexpr int i = m - 26; kf[i >> 2][i & 3] = lds128(ka[i & 3] + (i >> 2) * 4096u); }
      if constexpr (do_k && (m == 38 || m == 39)) { ka[2 * (m - 38)] += advc; ka[2 * (m - 38) + 1] += advc; pinv(ka[2 * (m - 38)]); pinv(ka[2 * (m - 38) + 1]); }
      if constexpr (mode == 0) {
        if constexpr (m == 26) setm0<0>(dK);
        if constexpr (m >= 27 && m < 31) pieceK<m - 27>(kv, sK);
        if constexpr (m == 28) { cV += 2 * fV; cV = cV == 10 ? 0u : cV; pin(cV); }
        if constexpr (m == 29) { dV = ldsb + cV * UNIT + (unsigned)w * 4096u; tV += fV; soV = qb + (tV & 15u) * 8192u; pin(dV); pin(tV); pin(soV); }
        if constexpr (m == 30) { fV = (VMASK >> (t + 1)) & 1u; pin(fV); }
        if constexpr (m == 31) { sV = soV | ((fV - 1u) & OOB); pin(sV); }
        if constexpr (m == 43) { cK += 2 * fK; cK = cK == 11 ? 1u : cK; tK += fK; soK = qb + (tK & 15u) * 8192u; pin(cK); pin(tK); pin(soK); }
        if constexpr (m == 44) { dK = ldsb + cK * UNIT + (unsigned)w * 4096u; fK = (KMASK >> (t + 1)) & 1u; sK = soK | ((fK - 1u) & OOB); pin(dK); pin(fK); pin(sK); }
      }
      if constexpr (mode == 1) { constexpr int i = rank_of(m, {26, 29, 32, 35, 38, 41, 43}); if constexpr (i >= 0) wo_set(IC<8 + (i < 0 ? 0 : i)>{});
                                 constexpr int k = rank_of(m, {27, 30, 33, 36, 39, 42, 44}); if constexpr (k >= 0) wo_ld(IC<8 + (k < 0 ? 0 : k)>{}); }
      if constexpr (m == 35) { slotV = slotV == 8 ? 0 : slotV + 2; pin(slotV); }
      if constexpr (m == 36) { adv = slotV == 8 ? (unsigned)(-8 * (int)UNIT) : 2 * UNIT; pin(adv); }
      GAP();
    });
    if constexpr (do_qk) {
      if (__builtin_expect(bal != 0, 0)) {
        nop16();
        sfor<5>([&](auto QT) INL { constexpr int qt = QT;
          float m = max3f(max3f(Sn[0][qt][0], Sn[0][qt][1], Sn[0][qt][2]), max3f(Sn[0][qt][3], Sn[1][qt][0], Sn[1][qt][1]), fmaxf(Sn[1][qt][2], Sn[1][qt][3]));
          m = fmaxf(xmax16_32(m), 0.f);
          const float f = __builtin_amdgcn_exp2f(-m);
          negm[qt] -= m; Sn[0][qt] -= m; Sn[1][qt] -= m;
          if constexpr (qt < 3) ascale<AL0 + 4 * qt>(f); else if constexpr (qt == 3) L3 *= f; else L4 *= f;
          sfor<8>([&](auto DT) INL { ascale<AO(DT, qt)>(f); }); });
        asm volatile("s_nop 1" ::: "memory");
        GAP();
      }
    }
  };
  using T = std::true_type; using F = std::false_type;
  iter(0, S0, S1, T{}, T{}, T{}, IC<0>{}, IC<vm_top(0)>{});
  for (int t = 1; t < 13; t += 2) { iter(t, S1, S0, T{}, T{}, F{}, IC<0>{}, IC<vm_loop()>{}); iter(t + 1, S0, S1, T{}, T{}, F{}, IC<0>{}, IC<vm_loop()>{}); }
#pragma unroll
  for (int n = 0; n < 5; ++n) { int c = 64 * (op.wpi0 + n) + lane; op.voffWo[n] = (c / 18) * 1152u + (c % 18) * 16u - (n & 3) * 1024u; }
  iter(13, S1, S0, T{}, T{}, F{}, IC<1>{}, IC<vm_top(13)>{});
  iter(14, S0, S1, T{}, F{}, F{}, IC<2>{}, IC<vm_top(14)>{});
  iter(15, S1, S0, F{}, F{}, F{}, IC<3>{}, IC<vm_top(15)>{});
  RST(6);
  wait_bar<0>();
  RST(21);
  asm volatile("" : "+v"(ack));
  asm volatile("s_nop 7" ::: "memory");
  u32x4 late[10];
  const unsigned gb = (unsigned)(((br * 8 + b) * 32 + h0 + hh) * 40 + 5 * rh) * 1024u;
  {
    const unsigned lb = ldsb + (unsigned)rh * 5120u + lane * 16u + (hh ? OBUF[2] : OBUF[0]), lb2 = ldsb + (unsigned)rh * 5120u + lane * 16u + (hh ? OBUF[3] : OBUF[1]);
    float inv[5];
    sfor<5>([&](auto QT) INL { constexpr int qt = QT;
      float l; if constexpr (qt < 3) l = aread<AL0 + 4 * qt>()[0]; else if constexpr (qt == 3) l = L3[0]; else l = L4[0];
      inv[qt] = 1.0f / l; });
    sfor<20>([&](auto I) INL { constexpr int s = I / 5, qt = I % 5;
      const u32x4 fr = __builtin_bit_cast(u32x4, pack8(aread<AO(2 * s, qt)>() * inv[qt], aread<AO(2 * s + 1, qt)>() * inv[qt]));
      if constexpr (s < 2) __builtin_amdgcn_raw_buffer_store_b128(fr, op.o, lane * 16u + (s * 10 + qt) * 1024u, gb, 16); else late[I - 10] = fr;
      *(AS3 u32x4*)(size_t)((s < 2 ? lb : lb2) + (s & 1) * 10240u + qt * 1024u) = fr; });
  }
  RST(7);
  wait_bar<10>();
  RST(8);
  own_acc();
  if (w & 1) oproj<1>(RSA ldsb, w, lane, bid, op, ws, smp, ack, outp, late, gb);
  else oproj<0>(RSA ldsb, w, lane, bid, op, ws, smp, ack, outp, late, gb);
  asm volatile("fused_code_end:");
}

extern "C" void kernel_launch(void* const* d_in, const int* in_sizes, int n_in, void* d_out, int out_size, void* d_ws, size_t ws_size, hipStream_t stream) {
  (void)hipFuncSetAttribute((const void*)fused_kernel, hipFuncAttributeMaxDynamicSharedMemorySize, 163840);
  fused_kernel<<<256, 256, 163840, stream>>>((const char*)d_in[0], (const char*)d_in[1], (const char*)d_in[2], (const char*)d_in[3], (const char*)d_in[4],
                                             (const char*)d_in[5], (const char*)d_in[6], (const char*)d_in[7], (const char*)d_in[8], (const char*)d_in[9],
                                             (char*)d_out, (char*)d_ws);
}
```
